```python
import torch

def _softmax(x, axis=-1):
    return torch.softmax(x, dim=axis)
def _gelu(x):
    return torch.nn.functional.gelu(x, approximate="tanh")
def _silu(x):
    return torch.nn.functional.silu(x)
def _sigmoid(x):
    return torch.sigmoid(x)
def _layernorm(x, axis=-1, eps=1e-5):
    m = x.mean(dim=axis, keepdim=True)
    v = x.var(dim=axis, keepdim=True, unbiased=False)
    return (x - m) / torch.sqrt(v + eps)
def _scatter_add(tgt, idx, src, axis=0):
    return tgt.clone().index_add_(axis, idx, src)
def _segment_sum(src, seg, n):
    out = torch.zeros((n,) + src.shape[1:], dtype=src.dtype)
    return out.index_add_(0, seg, src)
def _pad(x, pw):
    flat = tuple(v for pair in reversed(pw) for v in pair)
    return torch.nn.functional.pad(x, flat)

kernel_name = 'interleave_layers__mha_block__glu_gated_swiglu__prompt'

def setup_inputs():

    _w = (lambda a: (lambda t: t.to(torch.bfloat16) if t.is_floating_point() else t)(torch.as_tensor(a)))
    def _A_setup():
        B, S, D, H = 1, 2048, 448, 16
        return {
            "x": _w((lambda *s: torch.randn(*s, dtype=torch.bfloat16) * 0.05)(B, S, D)),
            "wqkv": _w((lambda *s: torch.randn(*s, dtype=torch.bfloat16) * 0.05)(D, 3 * D)),
            "wo": _w((lambda *s: torch.randn(*s, dtype=torch.bfloat16) * 0.05)(D, D)),
        }
    _A_in = _A_setup()

    def _B_setup():
        B, S, D, F = 1, 2048, 448, 1792
        return {
            "x": _w((lambda *s: torch.randn(*s, dtype=torch.bfloat16) * 0.05)(B, S, D)),
            "w1": _w((lambda *s: torch.randn(*s, dtype=torch.bfloat16) * 0.4)(D, F) * (2.0 / D) ** 0.5),
            "w3": _w((lambda *s: torch.randn(*s, dtype=torch.bfloat16) * 0.4)(D, F) * (2.0 / D) ** 0.5),
            "w2": _w((lambda *s: torch.randn(*s, dtype=torch.bfloat16) * 0.4)(F, D) * (2.0 / F) ** 0.5),
        }
    _B_in = _B_setup()

    return {
        "A_x": _A_in["x"],
        "A_wqkv": _A_in["wqkv"],
        "A_wo": _A_in["wo"],
        "B_x": _B_in["x"],
        "B_w1": _B_in["w1"],
        "B_w3": _B_in["w3"],
        "B_w2": _B_in["w2"],
    }

def reference(A_x, A_wqkv, A_wo, B_x, B_w1, B_w3, B_w2):
    def _A_ref(x, wqkv, wo):
        B, S, D = x.shape
        H = 16
        Dh = D // H
        qkv = (lambda x, *d: x.permute(*d))((x @ wqkv).reshape(B, S, 3, H, Dh), 2, 0, 3, 1, 4)
        q, k, v = qkv[0], qkv[1], qkv[2]
        scores = torch.einsum("bhqd,bhkd->bhqk", q, k) * (Dh ** -0.5)
        p = _softmax(scores, axis=-1)
        o = torch.einsum("bhqk,bhkd->bhqd", p, v)
        return (lambda x, *d: x.permute(*d))(o, 0, 2, 1, 3).reshape(B, S, D) @ wo

    def _B_ref(x, w1, w3, w2):
        gate = torch.einsum("bsd,df->bsf", x, w1)
        up = torch.einsum("bsd,df->bsf", x, w3)
        h = _silu(gate) * up
        out = torch.einsum("bsf,fd->bsd", h, w2)
        return out

    x = A_x
    x = x + _A_ref(_layernorm(x, axis=-1), A_wqkv, A_wo)
    x = x + _B_ref(_layernorm(x, axis=-1), B_w1, B_w3, B_w2)
    x = x + _A_ref(_layernorm(x, axis=-1), A_wqkv, A_wo)
    x = x + _B_ref(_layernorm(x, axis=-1), B_w1, B_w3, B_w2)
    return x
```

```cpp
#include <hip/hip_runtime.h>
#include <hip/hip_bf16.h>
#include <stdint.h>


#define S_LEN 2048
#define DM 448
#define NH 16
#define DH 28
#define FF 1792

typedef __attribute__((ext_vector_type(8))) short bf16x8;
typedef __attribute__((ext_vector_type(4))) short s16x4;
typedef __attribute__((ext_vector_type(16))) float f32x16;
typedef __attribute__((ext_vector_type(4))) unsigned u32x4;
typedef __attribute__((ext_vector_type(2))) unsigned u32x2;
typedef unsigned short u16;

#define DI __device__ __forceinline__
#define MFMA(a, b, c) __builtin_amdgcn_mfma_f32_32x32x16_bf16((a), (b), (c), 0, 0, 0)

DI float bf2f(u16 v) { return __uint_as_float(((unsigned)v) << 16); }
DI u16 f2bf(float f) {
  __hip_bfloat16 b = __float2bfloat16(f);
  return *reinterpret_cast<u16*>(&b);
}
DI float bfr(float f) { return bf2f(f2bf(f)); }
DI unsigned pack2(float lo, float hi) { return (unsigned)f2bf(lo) | ((unsigned)f2bf(hi) << 16); }

DI s16x4 tr_read(const char* p) {
  return __builtin_amdgcn_ds_read_tr16_b64_v4i16(
      (__attribute__((address_space(3))) s16x4*)(p));
}
DI bf16x8 tr_frag(const char* p, int stride) {
  s16x4 lo = tr_read(p);
  s16x4 hi = tr_read(p + 4 * stride);
  return __builtin_shufflevector(lo, hi, 0, 1, 2, 3, 4, 5, 6, 7);
}
DI bf16x8 lds_b128(const char* p) { return *reinterpret_cast<const bf16x8*>(p); }

template <int BM, int SA>
DI void ln_rows_to_lds(const u16* __restrict__ x, int m0, char* sA, int tid) {
  const int sub = tid & 7;
#pragma unroll
  for (int pass = 0; pass < BM / 32; ++pass) {
    const int row = pass * 32 + (tid >> 3);
    const u16* xr = x + (size_t)(m0 + row) * DM;
    u32x4 v[7];
#pragma unroll
    for (int c = 0; c < 7; ++c) v[c] = *reinterpret_cast<const u32x4*>(xr + (c * 8 + sub) * 8);
    float f[56];
    float sum = 0.f;
#pragma unroll
    for (int c = 0; c < 7; ++c)
#pragma unroll
      for (int j = 0; j < 4; ++j) {
        f[c * 8 + 2 * j] = __uint_as_float(v[c][j] << 16);
        f[c * 8 + 2 * j + 1] = __uint_as_float(v[c][j] & 0xffff0000u);
        sum += f[c * 8 + 2 * j] + f[c * 8 + 2 * j + 1];
      }
    sum += __shfl_xor(sum, 1);
    sum += __shfl_xor(sum, 2);
    sum += __shfl_xor(sum, 4);
    const float mean = sum * (1.0f / DM);
    float sq = 0.f;
#pragma unroll
    for (int i = 0; i < 56; ++i) {
      const float d = f[i] - mean;
      sq += d * d;
    }
    sq += __shfl_xor(sq, 1);
    sq += __shfl_xor(sq, 2);
    sq += __shfl_xor(sq, 4);
    const float mean_b = bfr(mean);
    const float var_b = bfr(sq * (1.0f / DM));
    const float sd = bfr(sqrtf(bfr(var_b + 1e-5f)));
    const float inv = 1.0f / sd;
#pragma unroll
    for (int c = 0; c < 7; ++c) {
      u32x4 o;
#pragma unroll
      for (int j = 0; j < 4; ++j)
        o[j] = pack2(bfr(f[c * 8 + 2 * j] - mean_b) * inv, bfr(f[c * 8 + 2 * j + 1] - mean_b) * inv);
      *reinterpret_cast<u32x4*>(sA + row * SA + (c * 8 + sub) * 16) = o;
    }
  }
}

#define QKV_SA 912
#define QKV_SB 192
__global__ __launch_bounds__(256) void k_ln_qkv(const u16* __restrict__ x, const u16* __restrict__ w,
                                                u16* __restrict__ Qp, u16* __restrict__ Kp,
                                                u16* __restrict__ Vt) {
  extern __shared__ __attribute__((aligned(16))) char smem[];
  char* sA = smem;
  char* sB = smem + 128 * QKV_SA;
  const int tid = threadIdx.x, lane = tid & 63, wv = tid >> 6;
  const int bm = blockIdx.x & 15, bn = blockIdx.x >> 4;
  const int m0 = bm * 128, n0 = bn * 96;

  u32x4 br[3];
  auto loadB = [&](int kt) {
#pragma unroll
    for (int i = 0; i < 3; ++i) {
      const int c = tid + 256 * i, r = c / 12, c16 = c % 12;
      br[i] = *reinterpret_cast<const u32x4*>(w + (size_t)(kt * 64 + r) * (3 * DM) + n0 + c16 * 8);
    }
  };
  auto storeB = [&](int buf) {
#pragma unroll
    for (int i = 0; i < 3; ++i) {
      const int c = tid + 256 * i, r = c / 12, c16 = c % 12;
      *reinterpret_cast<u32x4*>(sB + buf * (64 * QKV_SB) + r * QKV_SB + c16 * 16) = br[i];
    }
  };
  loadB(0);
  ln_rows_to_lds<128, QKV_SA>(x, m0, sA, tid);
  storeB(0);
  __syncthreads();

  f32x16 acc[3];
#pragma unroll
  for (int t = 0; t < 3; ++t)
#pragma unroll
    for (int i = 0; i < 16; ++i) acc[t][i] = 0.f;

  const int l31 = lane & 31, h = lane >> 5, i16 = lane & 15, q4 = i16 >> 2, p4 = i16 & 3,
            blk = (lane >> 4) & 1;
  const char* aBase = sA + (wv * 32 + l31) * QKV_SA + 16 * h;
  const char* bBase = sB + (8 * h + q4) * QKV_SB + (16 * blk + 4 * p4) * 2;

#pragma unroll 1
  for (int kt = 0; kt < 7; ++kt) {
    if (kt < 6) loadB(kt + 1);
    const char* bb = bBase + (kt & 1) * (64 * QKV_SB);
#pragma unroll
    for (int ks = 0; ks < 4; ++ks) {
      const bf16x8 xf = lds_b128(aBase + (kt * 64 + ks * 16) * 2);
#pragma unroll
      for (int t = 0; t < 3; ++t) {
        const bf16x8 wf = tr_frag(bb + ks * 16 * QKV_SB + t * 64, QKV_SB);
        if (n0 + 32 * t >= 2 * DM)
          acc[t] = MFMA(xf, wf, acc[t]);
        else
          acc[t] = MFMA(wf, xf, acc[t]);
      }
    }
    if (kt < 6) storeB((kt + 1) & 1);
    __syncthreads();
  }

#pragma unroll
  for (int t = 0; t < 3; ++t) {
    const int ns = n0 + 32 * t;
    if (ns < 2 * DM) {
      u16* dst = (ns < DM) ? Qp : Kp;
      const int cb = ns % DM;
      const int m = m0 + wv * 32 + l31;
#pragma unroll
      for (int g = 0; g < 4; ++g) {
        const int c = cb + 8 * g + 4 * h;
        const int head = c / DH, d = c % DH;
        u32x2 o;
        o[0] = pack2(acc[t][4 * g], acc[t][4 * g + 1]);
        o[1] = pack2(acc[t][4 * g + 2], acc[t][4 * g + 3]);
        u16* p = dst + ((size_t)head * S_LEN + m) * 32 + d;
        *reinterpret_cast<u32x2*>(p) = o;
        if (d == 24) *reinterpret_cast<u32x2*>(p + 4) = (u32x2){0u, 0u};
      }
    } else {
      const int c = ns - 2 * DM + l31;
      const int head = c / DH, d = c % DH;
#pragma unroll
      for (int g = 0; g < 4; ++g) {
        const int key = m0 + wv * 32 + 8 * g + 4 * h;
        const int pos = (key & ~15) + 8 * ((key >> 2) & 1) + 4 * ((key >> 3) & 1);
        u32x2 o;
        o[0] = pack2(acc[t][4 * g], acc[t][4 * g + 1]);
        o[1] = pack2(acc[t][4 * g + 2], acc[t][4 * g + 3]);
        u16* p = Vt + ((size_t)head * 32 + d) * S_LEN + pos;
        *reinterpret_cast<u32x2*>(p) = o;
        if (d == 27) {
          const u32x2 ones = (u32x2){0x3f803f80u, 0x3f803f80u};
          *reinterpret_cast<u32x2*>(p + 1 * S_LEN) = ones;
          *reinterpret_cast<u32x2*>(p + 2 * S_LEN) = ones;
          *reinterpret_cast<u32x2*>(p + 3 * S_LEN) = ones;
          *reinterpret_cast<u32x2*>(p + 4 * S_LEN) = ones;
        }
      }
    }
  }
}

#define AT_SK 80
#define AT_SV 272
#define AT_KB (128 * AT_SK)
#define AT_VB (32 * AT_SV)
#define AT_BUF (2 * AT_KB + 2 * AT_VB)
__global__ __launch_bounds__(512) void k_attn(const u16* __restrict__ Qp, const u16* __restrict__ Kp,
                                              const u16* __restrict__ Vt, u16* __restrict__ O) {
  extern __shared__ __attribute__((aligned(16))) char smem[];
  const int tid = threadIdx.x, lane = tid & 63, wv = tid >> 6;
  const int bid = blockIdx.x;
  const int head = (bid & 7) * 2 + ((bid >> 3) & 1), qt = bid >> 4;
  const int l31 = lane & 31, h = lane >> 5;
  const int qs = wv & 3, kh = wv >> 2;

  const u16* Kh = Kp + (size_t)head * S_LEN * 32;
  const u16* Vh = Vt + (size_t)head * 32 * S_LEN;

  u32x4 kr[2], vr[2];
  auto loadKV = [&](int it) {
#pragma unroll
    for (int i = 0; i < 2; ++i) {
      const int c = tid + 512 * i;
      const int hf = c >> 9, key = (c >> 2) & 127, part = c & 3;
      kr[i] = *reinterpret_cast<const u32x4*>(Kh + (size_t)(hf * 1024 + it * 128 + key) * 32 + part * 8);
      const int d = (c >> 4) & 31, p16 = c & 15;
      vr[i] = *reinterpret_cast<const u32x4*>(Vh + (size_t)d * S_LEN + hf * 1024 + it * 128 + p16 * 8);
    }
  };
  auto storeKV = [&](int buf) {
    char* b = smem + buf * AT_BUF;
#pragma unroll
    for (int i = 0; i < 2; ++i) {
      const int c = tid + 512 * i;
      const int hf = c >> 9, key = (c >> 2) & 127, part = c & 3;
      *reinterpret_cast<u32x4*>(b + hf * AT_KB + key * AT_SK + part * 16) = kr[i];
      const int d = (c >> 4) & 31, p16 = c & 15;
      *reinterpret_cast<u32x4*>(b + 2 * AT_KB + hf * AT_VB + d * AT_SV + p16 * 16) = vr[i];
    }
  };
  loadKV(0);

  const int qrow = qt * 128 + qs * 32 + l31;
  const u16* qp = Qp + ((size_t)head * S_LEN + qrow) * 32 + 8 * h;
  const bf16x8 qf0 = *reinterpret_cast<const bf16x8*>(qp);
  const bf16x8 qf1 = *reinterpret_cast<const bf16x8*>(qp + 16);

  storeKV(0);
  __syncthreads();

  const float cs = 0.18898223650461362f * 1.4426950408889634f;
  float m = -INFINITY;
  f32x16 o;
#pragma unroll
  for (int i = 0; i < 16; ++i) o[i] = 0.f;

#pragma unroll 1
  for (int it = 0; it < 8; ++it) {
    if (it < 7) loadKV(it + 1);
    const char* kb = smem + (it & 1) * AT_BUF + kh * AT_KB + l31 * AT_SK + 16 * h;
    const char* vb = smem + (it & 1) * AT_BUF + 2 * AT_KB + kh * AT_VB + l31 * AT_SV + 16 * h;
    f32x16 s[4];
#pragma unroll
    for (int t = 0; t < 4; ++t) {
      f32x16 z;
#pragma unroll
      for (int i = 0; i < 16; ++i) z[i] = 0.f;
      s[t] = MFMA(lds_b128(kb + t * 32 * AT_SK), qf0, z);
      s[t] = MFMA(lds_b128(kb + t * 32 * AT_SK + 32), qf1, s[t]);
    }
    float mx = s[0][0];
#pragma unroll
    for (int t = 0; t < 4; ++t)
#pragma unroll
      for (int i = 0; i < 16; ++i) mx = fmaxf(mx, s[t][i]);
    mx = fmaxf(mx, __shfl_xor(mx, 32));
    const float mnew = fmaxf(m, mx);
    const float alpha = __builtin_amdgcn_exp2f((m - mnew) * cs);
    m = mnew;
    const float mc = mnew * cs;
#pragma unroll
    for (int i = 0; i < 16; ++i) o[i] *= alpha;
#pragma unroll
    for (int t = 0; t < 4; ++t) {
#pragma unroll
      for (int sp = 0; sp < 2; ++sp) {
        u32x4 pk;
#pragma unroll
        for (int j = 0; j < 4; ++j) {
          const float p0 = __builtin_amdgcn_exp2f(fmaf(s[t][8 * sp + 2 * j], cs, -mc));
          const float p1 = __builtin_amdgcn_exp2f(fmaf(s[t][8 * sp + 2 * j + 1], cs, -mc));
          pk[j] = pack2(p0, p1);
        }
        const bf16x8 pf = __builtin_bit_cast(bf16x8, pk);
        o = MFMA(lds_b128(vb + (t * 32 + sp * 16) * 2), pf, o);
      }
    }
    if (it < 7) storeKV((it + 1) & 1);
    __syncthreads();
  }

  float* red = reinterpret_cast<float*>(smem);
  if (kh == 1) {
    red[(qs * 17 + 16) * 64 + lane] = m;
#pragma unroll
    for (int i = 0; i < 16; ++i) red[(qs * 17 + i) * 64 + lane] = o[i];
  }
  __syncthreads();
  if (kh == 0) {
    const float m2 = red[(qs * 17 + 16) * 64 + lane];
    const float mn = fmaxf(m, m2);
    const float a1 = __builtin_amdgcn_exp2f((m - mn) * cs);
    const float a2 = __builtin_amdgcn_exp2f((m2 - mn) * cs);
#pragma unroll
    for (int i = 0; i < 16; ++i) o[i] = o[i] * a1 + red[(qs * 17 + i) * 64 + lane] * a2;
    const float l = __shfl(o[12], l31 + 32);
    const float inv = 1.0f / l;
    u16* op = O + (size_t)qrow * DM + head * DH;
#pragma unroll
    for (int g = 0; g < 4; ++g) {
      const int d = 8 * g + 4 * h;
      if (d < DH) {
        u32x2 v;
        v[0] = pack2(o[4 * g] * inv, o[4 * g + 1] * inv);
        v[1] = pack2(o[4 * g + 2] * inv, o[4 * g + 3] * inv);
        *reinterpret_cast<u32x2*>(op + d) = v;
      }
    }
  }
}

#define GR_SA 464
#define GR_SB 192
#define GR_BUF (64 * GR_SA + 224 * GR_SB)
template <int KTOT>
__global__ __launch_bounds__(256) void k_gemm_res(const u16* __restrict__ A, const u16* __restrict__ W,
                                                  const u16* __restrict__ res, u16* __restrict__ out) {
  extern __shared__ __attribute__((aligned(16))) char smem[];
  const int tid = threadIdx.x, lane = tid & 63, wv = tid >> 6;
  const int bm = blockIdx.x & 31, bn = blockIdx.x >> 5;
  const int m0 = bm * 64, n0 = bn * 64;
  constexpr int NC = KTOT / 224;

  u32x4 ar[7], br[7];
  auto load = [&](int kc) {
#pragma unroll
    for (int i = 0; i < 7; ++i) {
      const int c = tid + 256 * i;
      const int r = c / 28, c16 = c % 28;
      ar[i] = *reinterpret_cast<const u32x4*>(A + (size_t)(m0 + r) * KTOT + kc * 224 + c16 * 8);
      const int rb = c >> 3, cb = c & 7;
      br[i] = *reinterpret_cast<const u32x4*>(W + (size_t)(kc * 224 + rb) * DM + n0 + cb * 8);
    }
  };
  auto store = [&](int buf) {
    char* b = smem + buf * GR_BUF;
#pragma unroll
    for (int i = 0; i < 7; ++i) {
      const int c = tid + 256 * i;
      const int r = c / 28, c16 = c % 28;
      *reinterpret_cast<u32x4*>(b + r * GR_SA + c16 * 16) = ar[i];
      const int rb = c >> 3, cb = c & 7;
      *reinterpret_cast<u32x4*>(b + 64 * GR_SA + rb * GR_SB + cb * 16) = br[i];
    }
  };
  load(0);
  store(0);
  __syncthreads();

  const int wm = wv & 1, wn = wv >> 1;
  const int l31 = lane & 31, h = lane >> 5, i16 = lane & 15, q4 = i16 >> 2, p4 = i16 & 3,
            blk = (lane >> 4) & 1;
  const int aOff = (wm * 32 + l31) * GR_SA + 16 * h;
  const int bOff = 64 * GR_SA + (8 * h + q4) * GR_SB + (wn * 32 + 16 * blk + 4 * p4) * 2;

  f32x16 acc;
#pragma unroll
  for (int i = 0; i < 16; ++i) acc[i] = 0.f;

#pragma unroll 1
  for (int kc = 0; kc < NC; ++kc) {
    if (kc + 1 < NC) load(kc + 1);
    const char* b = smem + (kc & 1) * GR_BUF;
#pragma unroll
    for (int ks = 0; ks < 14; ++ks) {
      const bf16x8 xf = lds_b128(b + aOff + ks * 32);
      const bf16x8 wf = tr_frag(b + bOff + ks * 16 * GR_SB, GR_SB);
      acc = MFMA(wf, xf, acc);
    }
    if (kc + 1 < NC) store((kc + 1) & 1);
    __syncthreads();
  }

  const int m = m0 + wm * 32 + l31;
#pragma unroll
  for (int g = 0; g < 4; ++g) {
    const size_t off = (size_t)m * DM + n0 + wn * 32 + 8 * g + 4 * h;
    const u32x2 r = *reinterpret_cast<const u32x2*>(res + off);
    u32x2 o;
    o[0] = pack2(__uint_as_float(r[0] << 16) + bfr(acc[4 * g]),
                 __uint_as_float(r[0] & 0xffff0000u) + bfr(acc[4 * g + 1]));
    o[1] = pack2(__uint_as_float(r[1] << 16) + bfr(acc[4 * g + 2]),
                 __uint_as_float(r[1] & 0xffff0000u) + bfr(acc[4 * g + 3]));
    *reinterpret_cast<u32x2*>(out + off) = o;
  }
}

#define UP_SA 912
#define UP_SB 320
#define UP_BUF (2 * 32 * UP_SB)
__global__ __launch_bounds__(256) void k_ln_up(const u16* __restrict__ x, const u16* __restrict__ w1,
                                               const u16* __restrict__ w3, u16* __restrict__ Hb) {
  extern __shared__ __attribute__((aligned(16))) char smem[];
  char* sA = smem;
  char* sB = smem + 128 * UP_SA;
  const int tid = threadIdx.x, lane = tid & 63, wv = tid >> 6;
  const int bm = blockIdx.x & 15, bn = blockIdx.x >> 4;
  const int m0 = bm * 128, n0 = bn * 128;

  u32x4 br[4];
  auto loadB = [&](int kt) {
#pragma unroll
    for (int i = 0; i < 2; ++i) {
      const int c = tid + 256 * i, r = c >> 4, c16 = c & 15;
      br[i] = *reinterpret_cast<const u32x4*>(w1 + (size_t)(kt * 32 + r) * FF + n0 + c16 * 8);
      br[2 + i] = *reinterpret_cast<const u32x4*>(w3 + (size_t)(kt * 32 + r) * FF + n0 + c16 * 8);
    }
  };
  auto storeB = [&](int buf) {
#pragma unroll
    for (int i = 0; i < 2; ++i) {
      const int c = tid + 256 * i, r = c >> 4, c16 = c & 15;
      *reinterpret_cast<u32x4*>(sB + buf * UP_BUF + r * UP_SB + c16 * 16) = br[i];
      *reinterpret_cast<u32x4*>(sB + buf * UP_BUF + 32 * UP_SB + r * UP_SB + c16 * 16) = br[2 + i];
    }
  };
  loadB(0);
  ln_rows_to_lds<128, UP_SA>(x, m0, sA, tid);
  storeB(0);
  __syncthreads();

  const int wm = wv & 1, wn = wv >> 1;
  const int l31 = lane & 31, h = lane >> 5, i16 = lane & 15, q4 = i16 >> 2, p4 = i16 & 3,
            blk = (lane >> 4) & 1;
  const char* aBase = sA + (wm * 64 + l31) * UP_SA + 16 * h;
  const char* bBase = sB + (8 * h + q4) * UP_SB + (wn * 64 + 16 * blk + 4 * p4) * 2;

  f32x16 acc[2][2][2];
#pragma unroll
  for (int a = 0; a < 2; ++a)
#pragma unroll
    for (int b = 0; b < 2; ++b)
#pragma unroll
      for (int c = 0; c < 2; ++c)
#pragma unroll
        for (int i = 0; i < 16; ++i) acc[a][b][c][i] = 0.f;

#pragma unroll 1
  for (int kt = 0; kt < 14; ++kt) {
    if (kt < 13) loadB(kt + 1);
    const char* bb = bBase + (kt & 1) * UP_BUF;
#pragma unroll
    for (int ks = 0; ks < 2; ++ks) {
      bf16x8 xf[2];
#pragma unroll
      for (int mt = 0; mt < 2; ++mt) xf[mt] = lds_b128(aBase + mt * 32 * UP_SA + (kt * 32 + ks * 16) * 2);
#pragma unroll
      for (int a = 0; a < 2; ++a)
#pragma unroll
        for (int nt = 0; nt < 2; ++nt) {
          const bf16x8 wf = tr_frag(bb + a * 32 * UP_SB + ks * 16 * UP_SB + nt * 64, UP_SB);
#pragma unroll
          for (int mt = 0; mt < 2; ++mt) acc[a][nt][mt] = MFMA(wf, xf[mt], acc[a][nt][mt]);
        }
    }
    if (kt < 13) storeB((kt + 1) & 1);
    __syncthreads();
  }

#pragma unroll
  for (int mt = 0; mt < 2; ++mt) {
    const int m = m0 + wm * 64 + mt * 32 + l31;
#pragma unroll
    for (int nt = 0; nt < 2; ++nt)
#pragma unroll
      for (int g = 0; g < 4; ++g) {
        float hv[4];
#pragma unroll
        for (int j = 0; j < 4; ++j) {
          const float gt = bfr(acc[0][nt][mt][4 * g + j]);
          const float up = bfr(acc[1][nt][mt][4 * g + j]);
          const float sl = bfr(gt / (1.0f + __expf(-gt)));
          hv[j] = sl * up;
        }
        u32x2 o;
        o[0] = pack2(hv[0], hv[1]);
        o[1] = pack2(hv[2], hv[3]);
        *reinterpret_cast<u32x2*>(Hb + (size_t)m * FF + n0 + wn * 64 + nt * 32 + 8 * g + 4 * h) = o;
      }
  }
}

extern "C" void kernel_launch(void* const* d_in, const int* in_sizes, int n_in, void* d_out, int out_size,
                              void* d_ws, size_t ws_size, hipStream_t stream) {
  const u16* x = (const u16*)d_in[0];
  const u16* wqkv = (const u16*)d_in[1];
  const u16* wo = (const u16*)d_in[2];
  const u16* w1 = (const u16*)d_in[4];
  const u16* w3 = (const u16*)d_in[5];
  const u16* w2 = (const u16*)d_in[6];
  u16* out = (u16*)d_out;

  char* ws = (char*)d_ws;
  u16* Qp = (u16*)(ws);
  u16* Kp = (u16*)(ws + (2u << 20));
  u16* Vt = (u16*)(ws + (4u << 20));
  u16* Ob = (u16*)(ws + (6u << 20));
  u16* x1 = (u16*)(ws + (8u << 20));
  u16* x2 = (u16*)(ws + (10u << 20));
  u16* x3 = (u16*)(ws + (12u << 20));
  u16* Hb = (u16*)(ws + (16u << 20));

  const int ldsQKV = 128 * QKV_SA + 2 * 64 * QKV_SB;
  const int ldsAT = 2 * AT_BUF;
  const int ldsGR = 2 * GR_BUF;
  const int ldsUP = 128 * UP_SA + 2 * UP_BUF;
  {
    hipFuncSetAttribute((const void*)k_ln_qkv, hipFuncAttributeMaxDynamicSharedMemorySize, ldsQKV);
    hipFuncSetAttribute((const void*)k_attn, hipFuncAttributeMaxDynamicSharedMemorySize, ldsAT);
    hipFuncSetAttribute((const void*)k_gemm_res<448>, hipFuncAttributeMaxDynamicSharedMemorySize, ldsGR);
    hipFuncSetAttribute((const void*)k_gemm_res<1792>, hipFuncAttributeMaxDynamicSharedMemorySize, ldsGR);
    hipFuncSetAttribute((const void*)k_ln_up, hipFuncAttributeMaxDynamicSharedMemorySize, ldsUP);
  }

  const u16* xin = x;
  for (int layer = 0; layer < 2; ++layer) {
    u16* xa = layer == 0 ? x1 : x3;
    u16* xb = layer == 0 ? x2 : out;
    k_ln_qkv<<<224, 256, ldsQKV, stream>>>(xin, wqkv, Qp, Kp, Vt);
    k_attn<<<256, 512, ldsAT, stream>>>(Qp, Kp, Vt, Ob);
    k_gemm_res<448><<<224, 256, ldsGR, stream>>>(Ob, wo, xin, xa);
    k_ln_up<<<224, 256, ldsUP, stream>>>(xa, w1, w3, Hb);
    k_gemm_res<1792><<<224, 256, ldsGR, stream>>>(Hb, w2, xa, xb);
    xin = xb;
  }
}
```

```cpp
#include <hip/hip_runtime.h>
#include <hip/hip_bf16.h>
#include <stdint.h>


#define S_LEN 2048
#define DM 448
#define NH 16
#define DH 28
#define FF 1792

typedef __attribute__((ext_vector_type(8))) short bf16x8;
typedef __attribute__((ext_vector_type(4))) short s16x4;
typedef __attribute__((ext_vector_type(16))) float f32x16;
typedef __attribute__((ext_vector_type(4))) unsigned u32x4;
typedef __attribute__((ext_vector_type(2))) unsigned u32x2;
typedef unsigned short u16;

#define DI __device__ __forceinline__
#define MFMA(a, b, c) __builtin_amdgcn_mfma_f32_32x32x16_bf16((a), (b), (c), 0, 0, 0)

DI float bf2f(u16 v) { return __uint_as_float(((unsigned)v) << 16); }
DI u16 f2bf(float f) {
  __hip_bfloat16 b = __float2bfloat16(f);
  return *reinterpret_cast<u16*>(&b);
}
DI float bfr(float f) { return bf2f(f2bf(f)); }
DI unsigned pack2(float lo, float hi) { return (unsigned)f2bf(lo) | ((unsigned)f2bf(hi) << 16); }

DI s16x4 tr_read(const char* p) {
  return __builtin_amdgcn_ds_read_tr16_b64_v4i16(
      (__attribute__((address_space(3))) s16x4*)(p));
}
DI bf16x8 tr_frag(const char* p, int stride) {
  s16x4 lo = tr_read(p);
  s16x4 hi = tr_read(p + 4 * stride);
  return __builtin_shufflevector(lo, hi, 0, 1, 2, 3, 4, 5, 6, 7);
}
DI bf16x8 lds_b128(const char* p) { return *reinterpret_cast<const bf16x8*>(p); }

struct LnRegs { u32x4 v[2][7]; };
DI void ln_load(LnRegs& r, const u16* __restrict__ x, int m0, int tid) {
  const int sub = tid & 7;
#pragma unroll
  for (int pass = 0; pass < 2; ++pass) {
    const u16* xr = x + (size_t)(m0 + pass * 64 + (tid >> 3)) * DM;
#pragma unroll
    for (int c = 0; c < 7; ++c) r.v[pass][c] = *reinterpret_cast<const u32x4*>(xr + (c * 8 + sub) * 8);
  }
}
template <int SA>
DI void ln_store(const LnRegs& r, char* sA, int tid) {
  const int sub = tid & 7;
#pragma unroll
  for (int pass = 0; pass < 2; ++pass) {
    const int row = pass * 64 + (tid >> 3);
    float f[56];
    float sum = 0.f;
#pragma unroll
    for (int c = 0; c < 7; ++c)
#pragma unroll
      for (int j = 0; j < 4; ++j) {
        f[c * 8 + 2 * j] = __uint_as_float(r.v[pass][c][j] << 16);
        f[c * 8 + 2 * j + 1] = __uint_as_float(r.v[pass][c][j] & 0xffff0000u);
        sum += f[c * 8 + 2 * j] + f[c * 8 + 2 * j + 1];
      }
    sum += __shfl_xor(sum, 1);
    sum += __shfl_xor(sum, 2);
    sum += __shfl_xor(sum, 4);
    const float mean = sum * (1.0f / DM);
    float sq = 0.f;
#pragma unroll
    for (int i = 0; i < 56; ++i) {
      const float d = f[i] - mean;
      sq += d * d;
    }
    sq += __shfl_xor(sq, 1);
    sq += __shfl_xor(sq, 2);
    sq += __shfl_xor(sq, 4);
    const float mean_b = bfr(mean);
    const float var_b = bfr(sq * (1.0f / DM));
    const float sd = bfr(sqrtf(bfr(var_b + 1e-5f)));
    const float inv = 1.0f / sd;
#pragma unroll
    for (int c = 0; c < 7; ++c) {
      u32x4 o;
#pragma unroll
      for (int j = 0; j < 4; ++j)
        o[j] = pack2(bfr(f[c * 8 + 2 * j] - mean_b) * inv, bfr(f[c * 8 + 2 * j + 1] - mean_b) * inv);
      *reinterpret_cast<u32x4*>(sA + row * SA + (c * 8 + sub) * 16) = o;
    }
  }
}

#define QKV_SA 912
#define QKV_SB 192
__global__ __launch_bounds__(512) void k_ln_qkv(const u16* __restrict__ x, const u16* __restrict__ w,
                                                u16* __restrict__ QKV) {
  extern __shared__ __attribute__((aligned(16))) char smem[];
  char* sA = smem;
  char* sB = smem + 128 * QKV_SA;
  const int tid = threadIdx.x, lane = tid & 63, wv = tid >> 6;
  const int bm = blockIdx.x & 15, bn = blockIdx.x >> 4;
  const int m0 = bm * 128, n0 = bn * 96;
  const int wr = wv & 3, kp = wv >> 2;

  u32x4 br[11];
#pragma unroll
  for (int i = 0; i < 11; ++i) {
    const int c = tid + 512 * i, r = c / 12, c16 = c % 12;
    if (i < 10 || tid < 256)
      br[i] = *reinterpret_cast<const u32x4*>(w + (size_t)r * (3 * DM) + n0 + c16 * 8);
  }
  LnRegs lr;
  ln_load(lr, x, m0, tid);
  ln_store<QKV_SA>(lr, sA, tid);
#pragma unroll
  for (int i = 0; i < 6; ++i) {
    const int c = tid + 512 * i, r = c / 12, c16 = c % 12;
    if (c < 2688) *reinterpret_cast<u32x4*>(sB + r * QKV_SB + c16 * 16) = br[i];
  }
  __syncthreads();

  f32x16 acc[3];
#pragma unroll
  for (int t = 0; t < 3; ++t)
#pragma unroll
    for (int i = 0; i < 16; ++i) acc[t][i] = 0.f;

  const int l31 = lane & 31, h = lane >> 5, i16 = lane & 15, q4 = i16 >> 2, p4 = i16 & 3,
            blk = (lane >> 4) & 1;
  const char* aBase = sA + (wr * 32 + l31) * QKV_SA + 16 * h + kp * 32;
  const char* bBase = sB + (kp * 16 + 8 * h + q4) * QKV_SB + (16 * blk + 4 * p4) * 2;

#pragma unroll
  for (int half = 0; half < 2; ++half) {
    if (half == 1) {
      __syncthreads();
#pragma unroll
      for (int i = 5; i < 11; ++i) {
        const int c = tid + 512 * i, r = c / 12, c16 = c % 12;
        if (c >= 2688 && c < 5376) *reinterpret_cast<u32x4*>(sB + (r - 224) * QKV_SB + c16 * 16) = br[i];
      }
      __syncthreads();
    }
#pragma unroll
    for (int ks = 0; ks < 7; ++ks) {
      const bf16x8 xf = lds_b128(aBase + (half * 224 + ks * 32) * 2);
#pragma unroll
      for (int t = 0; t < 3; ++t) {
        const bf16x8 wf = tr_frag(bBase + ks * 32 * QKV_SB + t * 64, QKV_SB);
        acc[t] = MFMA(wf, xf, acc[t]);
      }
    }
  }

  __syncthreads();
  float* red = reinterpret_cast<float*>(smem);
  if (kp == 1) {
#pragma unroll
    for (int t = 0; t < 3; ++t)
#pragma unroll
      for (int i = 0; i < 16; ++i) red[((wr * 3 + t) * 16 + i) * 64 + lane] = acc[t][i];
  }
  __syncthreads();
  if (kp == 0) {
    const int m = m0 + wr * 32 + l31;
#pragma unroll
    for (int t = 0; t < 3; ++t) {
#pragma unroll
      for (int i = 0; i < 16; ++i) acc[t][i] += red[((wr * 3 + t) * 16 + i) * 64 + lane];
      const int ns = n0 + 32 * t;
      const int which = ns / DM, cb = ns % DM;
#pragma unroll
      for (int g = 0; g < 4; ++g) {
        const int c = cb + 8 * g + 4 * h;
        const int head = c / DH, d = c % DH;
        u32x2 o;
        o[0] = pack2(acc[t][4 * g], acc[t][4 * g + 1]);
        o[1] = pack2(acc[t][4 * g + 2], acc[t][4 * g + 3]);
        u16* p = QKV + ((size_t)(which * NH + head) * S_LEN + m) * 32 + d;
        *reinterpret_cast<u32x2*>(p) = o;
        if (d == 24) *reinterpret_cast<u32x2*>(p + 4) = (u32x2){which == 2 ? 0x3f80u : 0u, 0u};
      }
    }
  }
}

#define AT_SK 80
#define AT_SV 64
#define AT_KB (128 * AT_SK)
#define AT_VB (128 * AT_SV)
#define AT_BUF (2 * AT_KB + 2 * AT_VB)
__global__ __launch_bounds__(512) void k_attn(const u16* __restrict__ QKV, u16* __restrict__ O) {
  extern __shared__ __attribute__((aligned(16))) char smem[];
  const int tid = threadIdx.x, lane = tid & 63, wv = tid >> 6;
  const int bid = blockIdx.x;
  const int head = (bid & 7) * 2 + ((bid >> 3) & 1), qt = bid >> 4;
  const int l31 = lane & 31, h = lane >> 5, i16 = lane & 15, q4 = i16 >> 2, p4 = i16 & 3,
            blk = (lane >> 4) & 1;
  const int qs = wv & 3, kh = wv >> 2;

  const u16* Qh = QKV + (size_t)head * S_LEN * 32;
  const u16* Kh = QKV + (size_t)(NH + head) * S_LEN * 32;
  const u16* Vh = QKV + (size_t)(2 * NH + head) * S_LEN * 32;

  const int qrow = qt * 128 + qs * 32 + l31;
  const u16* qp = Qh + (size_t)qrow * 32 + 8 * h;
  const bf16x8 qf0 = *reinterpret_cast<const bf16x8*>(qp);
  const bf16x8 qf1 = *reinterpret_cast<const bf16x8*>(qp + 16);

  u32x4 kr[2], vr[2];
  auto loadKV = [&](int it) {
#pragma unroll
    for (int i = 0; i < 2; ++i) {
      const size_t off = (size_t)(i * 1024 + it * 128) * 32 + tid * 8;
      kr[i] = *reinterpret_cast<const u32x4*>(Kh + off);
      vr[i] = *reinterpret_cast<const u32x4*>(Vh + off);
    }
  };
  auto storeKV = [&](int buf) {
    char* b = smem + buf * AT_BUF;
#pragma unroll
    for (int i = 0; i < 2; ++i) {
      *reinterpret_cast<u32x4*>(b + i * AT_KB + (tid >> 2) * AT_SK + (tid & 3) * 16) = kr[i];
      *reinterpret_cast<u32x4*>(b + 2 * AT_KB + i * AT_VB + tid * 16) = vr[i];
    }
  };
  loadKV(0);
  storeKV(0);
  __syncthreads();

  const float cs = 0.18898223650461362f * 1.4426950408889634f;
  float m = -INFINITY;
  f32x16 o;
#pragma unroll
  for (int i = 0; i < 16; ++i) o[i] = 0.f;

#pragma unroll 1
  for (int it = 0; it < 8; ++it) {
    if (it < 7) loadKV(it + 1);
    const char* kb = smem + (it & 1) * AT_BUF + kh * AT_KB + l31 * AT_SK + 16 * h;
    const char* vb = smem + (it & 1) * AT_BUF + 2 * AT_KB + kh * AT_VB + (4 * h + q4) * AT_SV +
                     (16 * blk + 4 * p4) * 2;
    f32x16 s[4];
#pragma unroll
    for (int t = 0; t < 4; ++t) {
      f32x16 z;
#pragma unroll
      for (int i = 0; i < 16; ++i) z[i] = 0.f;
      s[t] = MFMA(lds_b128(kb + t * 32 * AT_SK), qf0, z);
      s[t] = MFMA(lds_b128(kb + t * 32 * AT_SK + 32), qf1, s[t]);
    }
    float mx = s[0][0];
#pragma unroll
    for (int t = 0; t < 4; ++t)
#pragma unroll
      for (int i = 0; i < 16; ++i) mx = fmaxf(mx, s[t][i]);
    mx = fmaxf(mx, __shfl_xor(mx, 32));
    const float mnew = fmaxf(m, mx);
    const float alpha = __builtin_amdgcn_exp2f((m - mnew) * cs);
    m = mnew;
    const float mc = mnew * cs;
#pragma unroll
    for (int i = 0; i < 16; ++i) o[i] *= alpha;
#pragma unroll
    for (int t = 0; t < 4; ++t) {
#pragma unroll
      for (int sp = 0; sp < 2; ++sp) {
        u32x4 pk;
#pragma unroll
        for (int j = 0; j < 4; ++j) {
          const float p0 = __builtin_amdgcn_exp2f(fmaf(s[t][8 * sp + 2 * j], cs, -mc));
          const float p1 = __builtin_amdgcn_exp2f(fmaf(s[t][8 * sp + 2 * j + 1], cs, -mc));
          pk[j] = pack2(p0, p1);
        }
        const bf16x8 pf = __builtin_bit_cast(bf16x8, pk);
        const s16x4 lo = tr_read(vb + (t * 32 + sp * 16) * AT_SV);
        const s16x4 hi = tr_read(vb + (t * 32 + sp * 16 + 8) * AT_SV);
        const bf16x8 vf = __builtin_shufflevector(lo, hi, 0, 1, 2, 3, 4, 5, 6, 7);
        o = MFMA(vf, pf, o);
      }
    }
    if (it < 7) storeKV((it + 1) & 1);
    __syncthreads();
  }

  float* red = reinterpret_cast<float*>(smem);
  if (kh == 1) {
    red[(qs * 17 + 16) * 64 + lane] = m;
#pragma unroll
    for (int i = 0; i < 16; ++i) red[(qs * 17 + i) * 64 + lane] = o[i];
  }
  __syncthreads();
  if (kh == 0) {
    const float m2 = red[(qs * 17 + 16) * 64 + lane];
    const float mn = fmaxf(m, m2);
    const float a1 = __builtin_amdgcn_exp2f((m - mn) * cs);
    const float a2 = __builtin_amdgcn_exp2f((m2 - mn) * cs);
#pragma unroll
    for (int i = 0; i < 16; ++i) o[i] = o[i] * a1 + red[(qs * 17 + i) * 64 + lane] * a2;
    const float l = __shfl(o[12], l31 + 32);
    const float inv = 1.0f / l;
    u16* op = O + (size_t)qrow * DM + head * DH;
#pragma unroll
    for (int g = 0; g < 4; ++g) {
      const int d = 8 * g + 4 * h;
      if (d < DH) {
        u32x2 v;
        v[0] = pack2(o[4 * g] * inv, o[4 * g + 1] * inv);
        v[1] = pack2(o[4 * g + 2] * inv, o[4 * g + 3] * inv);
        *reinterpret_cast<u32x2*>(op + d) = v;
      }
    }
  }
}

#define GR_SA 464
#define GR_SB 192
#define GR_BUF (64 * GR_SA + 224 * GR_SB)
template <int KTOT>
__global__ __launch_bounds__(512) void k_gemm_res(const u16* __restrict__ A, const u16* __restrict__ W,
                                                  const u16* __restrict__ res, u16* __restrict__ out) {
  extern __shared__ __attribute__((aligned(16))) char smem[];
  const int tid = threadIdx.x, lane = tid & 63, wv = tid >> 6;
  const int bm = blockIdx.x & 31, bn = blockIdx.x >> 5;
  const int m0 = bm * 64, n0 = bn * 64;
  constexpr int NC = KTOT / 224;

  u32x4 ar[4], br[4];
  auto load = [&](int kc) {
#pragma unroll
    for (int i = 0; i < 4; ++i) {
      const int c = tid + 512 * i;
      if (i < 3 || tid < 256) {
        const int r = c / 28, c16 = c % 28;
        ar[i] = *reinterpret_cast<const u32x4*>(A + (size_t)(m0 + r) * KTOT + kc * 224 + c16 * 8);
        const int rb = c >> 3, cb = c & 7;
        br[i] = *reinterpret_cast<const u32x4*>(W + (size_t)(kc * 224 + rb) * DM + n0 + cb * 8);
      }
    }
  };
  auto store = [&](int buf) {
    char* b = smem + buf * GR_BUF;
#pragma unroll
    for (int i = 0; i < 4; ++i) {
      const int c = tid + 512 * i;
      if (i < 3 || tid < 256) {
        const int r = c / 28, c16 = c % 28;
        *reinterpret_cast<u32x4*>(b + r * GR_SA + c16 * 16) = ar[i];
        const int rb = c >> 3, cb = c & 7;
        *reinterpret_cast<u32x4*>(b + 64 * GR_SA + rb * GR_SB + cb * 16) = br[i];
      }
    }
  };
  load(0);
  store(0);
  __syncthreads();

  const int wm = wv & 1, wn = (wv >> 1) & 1, kp = wv >> 2;
  const int l31 = lane & 31, h = lane >> 5, i16 = lane & 15, q4 = i16 >> 2, p4 = i16 & 3,
            blk = (lane >> 4) & 1;
  const int aOff = (wm * 32 + l31) * GR_SA + 16 * h + kp * 32;
  const int bOff = 64 * GR_SA + (kp * 16 + 8 * h + q4) * GR_SB + (wn * 32 + 16 * blk + 4 * p4) * 2;

  f32x16 acc;
#pragma unroll
  for (int i = 0; i < 16; ++i) acc[i] = 0.f;

#pragma unroll 1
  for (int kc = 0; kc < NC; ++kc) {
    if (kc + 1 < NC) load(kc + 1);
    const char* b = smem + (kc & 1) * GR_BUF;
#pragma unroll
    for (int ks = 0; ks < 7; ++ks) {
      const bf16x8 xf = lds_b128(b + aOff + ks * 64);
      const bf16x8 wf = tr_frag(b + bOff + ks * 32 * GR_SB, GR_SB);
      acc = MFMA(wf, xf, acc);
    }
    if (kc + 1 < NC) store((kc + 1) & 1);
    __syncthreads();
  }

  float* red = reinterpret_cast<float*>(smem);
  if (kp == 1) {
#pragma unroll
    for (int i = 0; i < 16; ++i) red[((wv & 3) * 16 + i) * 64 + lane] = acc[i];
  }
  __syncthreads();
  if (kp == 0) {
#pragma unroll
    for (int i = 0; i < 16; ++i) acc[i] += red[((wv & 3) * 16 + i) * 64 + lane];
    const int m = m0 + wm * 32 + l31;
#pragma unroll
    for (int g = 0; g < 4; ++g) {
      const size_t off = (size_t)m * DM + n0 + wn * 32 + 8 * g + 4 * h;
      const u32x2 r = *reinterpret_cast<const u32x2*>(res + off);
      u32x2 o;
      o[0] = pack2(__uint_as_float(r[0] << 16) + bfr(acc[4 * g]),
                   __uint_as_float(r[0] & 0xffff0000u) + bfr(acc[4 * g + 1]));
      o[1] = pack2(__uint_as_float(r[1] << 16) + bfr(acc[4 * g + 2]),
                   __uint_as_float(r[1] & 0xffff0000u) + bfr(acc[4 * g + 3]));
      *reinterpret_cast<u32x2*>(out + off) = o;
    }
  }
}

#define UP_SA 912
#define UP_SB 320
__global__ __launch_bounds__(512) void k_ln_up(const u16* __restrict__ x, const u16* __restrict__ w1,
                                               const u16* __restrict__ w3, u16* __restrict__ Hb) {
  extern __shared__ __attribute__((aligned(16))) char smem[];
  char* sA = smem;
  char* sB = smem + 128 * UP_SA;
  const int tid = threadIdx.x, lane = tid & 63, wv = tid >> 6;
  const int bm = blockIdx.x & 15, bn = blockIdx.x >> 4;
  const int m0 = bm * 128, n0 = bn * 128;

  u32x4 br[2][4];
  const int brow = tid >> 4, bc = tid & 15;
  auto loadB = [&](int kt, int slot) {
#pragma unroll
    for (int i = 0; i < 2; ++i) {
      const size_t off = (size_t)(kt * 64 + brow + 32 * i) * FF + n0 + bc * 8;
      br[slot][i] = *reinterpret_cast<const u32x4*>(w1 + off);
      br[slot][2 + i] = *reinterpret_cast<const u32x4*>(w3 + off);
    }
  };
  auto storeB = [&](int slot) {
#pragma unroll
    for (int i = 0; i < 2; ++i) {
      *reinterpret_cast<u32x4*>(sB + (brow + 32 * i) * UP_SB + bc * 16) = br[slot][i];
      *reinterpret_cast<u32x4*>(sB + 64 * UP_SB + (brow + 32 * i) * UP_SB + bc * 16) = br[slot][2 + i];
    }
  };
  loadB(0, 0);
  loadB(1, 1);
  LnRegs lr;
  ln_load(lr, x, m0, tid);
  ln_store<UP_SA>(lr, sA, tid);
  storeB(0);
  __syncthreads();

  const int wm = wv & 1, wn = (wv >> 1) & 1, kp = wv >> 2;
  const int l31 = lane & 31, h = lane >> 5, i16 = lane & 15, q4 = i16 >> 2, p4 = i16 & 3,
            blk = (lane >> 4) & 1;
  const char* aBase = sA + (wm * 64 + l31) * UP_SA + 16 * h + kp * 32;
  const char* bBase = sB + (kp * 16 + 8 * h + q4) * UP_SB + (wn * 64 + 16 * blk + 4 * p4) * 2;

  f32x16 acc[2][2][2];
#pragma unroll
  for (int a = 0; a < 2; ++a)
#pragma unroll
    for (int b = 0; b < 2; ++b)
#pragma unroll
      for (int c = 0; c < 2; ++c)
#pragma unroll
        for (int i = 0; i < 16; ++i) acc[a][b][c][i] = 0.f;

#pragma unroll
  for (int kt = 0; kt < 7; ++kt) {
#pragma unroll
    for (int ks = 0; ks < 2; ++ks) {
      bf16x8 xf[2];
#pragma unroll
      for (int mt = 0; mt < 2; ++mt) xf[mt] = lds_b128(aBase + mt * 32 * UP_SA + (kt * 64 + ks * 32) * 2);
#pragma unroll
      for (int a = 0; a < 2; ++a)
#pragma unroll
        for (int nt = 0; nt < 2; ++nt) {
          const bf16x8 wf = tr_frag(bBase + a * 64 * UP_SB + ks * 32 * UP_SB + nt * 64, UP_SB);
#pragma unroll
          for (int mt = 0; mt < 2; ++mt) acc[a][nt][mt] = MFMA(wf, xf[mt], acc[a][nt][mt]);
        }
    }
    if (kt < 6) {
      __syncthreads();
      storeB((kt + 1) & 1);
      if (kt < 5) loadB(kt + 2, kt & 1);
      __syncthreads();
    }
  }

  __syncthreads();
  float* red = reinterpret_cast<float*>(smem);
  if (kp == 1) {
#pragma unroll
    for (int a = 0; a < 2; ++a)
#pragma unroll
      for (int b = 0; b < 2; ++b)
#pragma unroll
        for (int c = 0; c < 2; ++c)
#pragma unroll
          for (int i = 0; i < 16; ++i)
            red[((((wv & 3) * 2 + a) * 2 + b) * 2 + c) * 1024 + i * 64 + lane] = acc[a][b][c][i];
  }
  __syncthreads();
  if (kp == 0) {
#pragma unroll
    for (int a = 0; a < 2; ++a)
#pragma unroll
      for (int b = 0; b < 2; ++b)
#pragma unroll
        for (int c = 0; c < 2; ++c)
#pragma unroll
          for (int i = 0; i < 16; ++i)
            acc[a][b][c][i] += red[((((wv & 3) * 2 + a) * 2 + b) * 2 + c) * 1024 + i * 64 + lane];
#pragma unroll
    for (int mt = 0; mt < 2; ++mt) {
      const int m = m0 + wm * 64 + mt * 32 + l31;
#pragma unroll
      for (int nt = 0; nt < 2; ++nt)
#pragma unroll
        for (int g = 0; g < 4; ++g) {
          float hv[4];
#pragma unroll
          for (int j = 0; j < 4; ++j) {
            const float gt = bfr(acc[0][nt][mt][4 * g + j]);
            const float up = bfr(acc[1][nt][mt][4 * g + j]);
            const float sl = bfr(gt / (1.0f + __expf(-gt)));
            hv[j] = sl * up;
          }
          u32x2 o;
          o[0] = pack2(hv[0], hv[1]);
          o[1] = pack2(hv[2], hv[3]);
          *reinterpret_cast<u32x2*>(Hb + (size_t)m * FF + n0 + wn * 64 + nt * 32 + 8 * g + 4 * h) = o;
        }
    }
  }
}

extern "C" void kernel_launch(void* const* d_in, const int* in_sizes, int n_in, void* d_out, int out_size,
                              void* d_ws, size_t ws_size, hipStream_t stream) {
  const u16* x = (const u16*)d_in[0];
  const u16* wqkv = (const u16*)d_in[1];
  const u16* wo = (const u16*)d_in[2];
  const u16* w1 = (const u16*)d_in[4];
  const u16* w3 = (const u16*)d_in[5];
  const u16* w2 = (const u16*)d_in[6];
  u16* out = (u16*)d_out;

  char* ws = (char*)d_ws;
  u16* QKV = (u16*)(ws);
  u16* Ob = (u16*)(ws + (6u << 20));
  u16* x1 = (u16*)(ws + (8u << 20));
  u16* x2 = (u16*)(ws + (10u << 20));
  u16* x3 = (u16*)(ws + (12u << 20));
  u16* Hb = (u16*)(ws + (16u << 20));

  const int ldsQKV = 128 * QKV_SA + 224 * QKV_SB;
  const int ldsAT = 2 * AT_BUF;
  const int ldsGR = 2 * GR_BUF;
  const int ldsUP = 128 * UP_SA + 2 * 64 * UP_SB;
  (void)hipFuncSetAttribute((const void*)k_ln_qkv, hipFuncAttributeMaxDynamicSharedMemorySize, ldsQKV);
  (void)hipFuncSetAttribute((const void*)k_attn, hipFuncAttributeMaxDynamicSharedMemorySize, ldsAT);
  (void)hipFuncSetAttribute((const void*)k_gemm_res<448>, hipFuncAttributeMaxDynamicSharedMemorySize, ldsGR);
  (void)hipFuncSetAttribute((const void*)k_gemm_res<1792>, hipFuncAttributeMaxDynamicSharedMemorySize, ldsGR);
  (void)hipFuncSetAttribute((const void*)k_ln_up, hipFuncAttributeMaxDynamicSharedMemorySize, ldsUP);

  const u16* xin = x;
  for (int layer = 0; layer < 2; ++layer) {
    u16* xa = layer == 0 ? x1 : x3;
    u16* xb = layer == 0 ? x2 : out;
    k_ln_qkv<<<224, 512, ldsQKV, stream>>>(xin, wqkv, QKV);
    k_attn<<<256, 512, ldsAT, stream>>>(QKV, Ob);
    k_gemm_res<448><<<224, 512, ldsGR, stream>>>(Ob, wo, xin, xa);
    k_ln_up<<<224, 512, ldsUP, stream>>>(xa, w1, w3, Hb);
    k_gemm_res<1792><<<224, 512, ldsGR, stream>>>(Hb, w2, xa, xb);
    xin = xb;
  }
}
```

```cpp
#include <hip/hip_runtime.h>
#include <hip/hip_bf16.h>
#include <stdint.h>


#define S_LEN 2048
#define DM 448
#define NH 16
#define DH 28
#define FF 1792

typedef __attribute__((ext_vector_type(8))) short bf16x8;
typedef __attribute__((ext_vector_type(4))) short s16x4;
typedef __attribute__((ext_vector_type(16))) float f32x16;
typedef __attribute__((ext_vector_type(4))) unsigned u32x4;
typedef __attribute__((ext_vector_type(2))) unsigned u32x2;
typedef unsigned short u16;

#define DI __device__ __forceinline__
#define MFMA(a, b, c) __builtin_amdgcn_mfma_f32_32x32x16_bf16((a), (b), (c), 0, 0, 0)

DI float bf2f(u16 v) { return __uint_as_float(((unsigned)v) << 16); }
DI u16 f2bf(float f) {
  __hip_bfloat16 b = __float2bfloat16(f);
  return *reinterpret_cast<u16*>(&b);
}
DI float bfr(float f) { return bf2f(f2bf(f)); }
DI unsigned pack2(float lo, float hi) { return (unsigned)f2bf(lo) | ((unsigned)f2bf(hi) << 16); }

DI s16x4 tr_read(const char* p) {
  return __builtin_amdgcn_ds_read_tr16_b64_v4i16(
      (__attribute__((address_space(3))) s16x4*)(p));
}
DI bf16x8 tr_frag(const char* p, int stride) {
  s16x4 lo = tr_read(p);
  s16x4 hi = tr_read(p + 4 * stride);
  return __builtin_shufflevector(lo, hi, 0, 1, 2, 3, 4, 5, 6, 7);
}
DI bf16x8 lds_b128(const char* p) { return *reinterpret_cast<const bf16x8*>(p); }

struct LnRegs { u32x4 v[2][7]; };
DI void ln_load(LnRegs& r, const u16* __restrict__ x, int m0, int tid) {
  const int sub = tid & 7;
#pragma unroll
  for (int pass = 0; pass < 2; ++pass) {
    const u16* xr = x + (size_t)(m0 + pass * 64 + (tid >> 3)) * DM;
#pragma unroll
    for (int c = 0; c < 7; ++c) r.v[pass][c] = *reinterpret_cast<const u32x4*>(xr + (c * 8 + sub) * 8);
  }
}
template <int SA>
DI void ln_store(const LnRegs& r, char* sA, float* sinv, int tid) {
  const int sub = tid & 7;
#pragma unroll
  for (int pass = 0; pass < 2; ++pass) {
    const int row = pass * 64 + (tid >> 3);
    float sum = 0.f;
#pragma unroll
    for (int c = 0; c < 7; ++c)
#pragma unroll
      for (int j = 0; j < 4; ++j)
        sum += __uint_as_float(r.v[pass][c][j] << 16) + __uint_as_float(r.v[pass][c][j] & 0xffff0000u);
    sum += __shfl_xor(sum, 1);
    sum += __shfl_xor(sum, 2);
    sum += __shfl_xor(sum, 4);
    const float mean = sum * (1.0f / DM);
    const float mean_b = bfr(mean);
    float sq = 0.f;
#pragma unroll
    for (int c = 0; c < 7; ++c) {
      u32x4 o;
#pragma unroll
      for (int j = 0; j < 4; ++j) {
        const float f0 = __uint_as_float(r.v[pass][c][j] << 16);
        const float f1 = __uint_as_float(r.v[pass][c][j] & 0xffff0000u);
        const float d0 = f0 - mean, d1 = f1 - mean;
        sq = fmaf(d0, d0, fmaf(d1, d1, sq));
        o[j] = pack2(f0 - mean_b, f1 - mean_b);
      }
      *reinterpret_cast<u32x4*>(sA + row * SA + (c * 8 + sub) * 16) = o;
    }
    sq += __shfl_xor(sq, 1);
    sq += __shfl_xor(sq, 2);
    sq += __shfl_xor(sq, 4);
    if (sub == 0) sinv[row] = 1.0f / bfr(sqrtf(bfr(bfr(sq * (1.0f / DM)) + 1e-5f)));
  }
}

#define QKV_SA 912
#define QKV_SB 192
__global__ __launch_bounds__(512) void k_ln_qkv(const u16* __restrict__ x, const u16* __restrict__ w,
                                                u16* __restrict__ QKV) {
  extern __shared__ __attribute__((aligned(16))) char smem[];
  char* sA = smem;
  char* sB = smem + 128 * QKV_SA;
  float* sinv = reinterpret_cast<float*>(smem + 128 * QKV_SA + 224 * QKV_SB);
  const int tid = threadIdx.x, lane = tid & 63, wv = tid >> 6;
  const int bm = blockIdx.x & 15, bn = blockIdx.x >> 4;
  const int m0 = bm * 128, n0 = bn * 96;
  const int wr = wv & 3, kp = wv >> 2;

  u32x4 br[11];
#pragma unroll
  for (int i = 0; i < 11; ++i) {
    const int c = tid + 512 * i, r = c / 12, c16 = c % 12;
    if (i < 10 || tid < 256)
      br[i] = *reinterpret_cast<const u32x4*>(w + (size_t)r * (3 * DM) + n0 + c16 * 8);
  }
  LnRegs lr;
  ln_load(lr, x, m0, tid);
  ln_store<QKV_SA>(lr, sA, sinv, tid);
#pragma unroll
  for (int i = 0; i < 6; ++i) {
    const int c = tid + 512 * i, r = c / 12, c16 = c % 12;
    if (c < 2688) *reinterpret_cast<u32x4*>(sB + r * QKV_SB + c16 * 16) = br[i];
  }
  __syncthreads();

  f32x16 acc[3];
#pragma unroll
  for (int t = 0; t < 3; ++t)
#pragma unroll
    for (int i = 0; i < 16; ++i) acc[t][i] = 0.f;

  const int l31 = lane & 31, h = lane >> 5, i16 = lane & 15, q4 = i16 >> 2, p4 = i16 & 3,
            blk = (lane >> 4) & 1;
  const char* aBase = sA + (wr * 32 + l31) * QKV_SA + 16 * h + kp * 32;
  const char* bBase = sB + (kp * 16 + 8 * h + q4) * QKV_SB + (16 * blk + 4 * p4) * 2;

#pragma unroll
  for (int half = 0; half < 2; ++half) {
    if (half == 1) {
      __syncthreads();
#pragma unroll
      for (int i = 5; i < 11; ++i) {
        const int c = tid + 512 * i, r = c / 12, c16 = c % 12;
        if (c >= 2688 && c < 5376) *reinterpret_cast<u32x4*>(sB + (r - 224) * QKV_SB + c16 * 16) = br[i];
      }
      __syncthreads();
    }
#pragma unroll
    for (int ks = 0; ks < 7; ++ks) {
      const bf16x8 xf = lds_b128(aBase + (half * 224 + ks * 32) * 2);
#pragma unroll
      for (int t = 0; t < 3; ++t) {
        const bf16x8 wf = tr_frag(bBase + ks * 32 * QKV_SB + t * 64, QKV_SB);
        acc[t] = MFMA(wf, xf, acc[t]);
      }
    }
  }

  __syncthreads();
  float* red = reinterpret_cast<float*>(sB);
  red = reinterpret_cast<float*>(sA);
  if (kp == 1) {
#pragma unroll
    for (int t = 0; t < 3; ++t)
#pragma unroll
      for (int i = 0; i < 16; ++i) red[((wr * 3 + t) * 16 + i) * 64 + lane] = acc[t][i];
  }
  __syncthreads();
  if (kp == 0) {
    const int m = m0 + wr * 32 + l31;
    const float inv = sinv[wr * 32 + l31];
#pragma unroll
    for (int t = 0; t < 3; ++t) {
      const int ns = n0 + 32 * t;
      const int which = ns / DM, cb = ns % DM;
      const float sc = which == 0 ? inv * (0.18898223650461362f * 1.4426950408889634f) : inv;
#pragma unroll
      for (int i = 0; i < 16; ++i) acc[t][i] = (acc[t][i] + red[((wr * 3 + t) * 16 + i) * 64 + lane]) * sc;
#pragma unroll
      for (int g = 0; g < 4; ++g) {
        const int c = cb + 8 * g + 4 * h;
        const int head = c / DH, d = c % DH;
        u32x2 o;
        o[0] = pack2(acc[t][4 * g], acc[t][4 * g + 1]);
        o[1] = pack2(acc[t][4 * g + 2], acc[t][4 * g + 3]);
        u16* p = QKV + ((size_t)(which * NH + head) * S_LEN + m) * 32 + d;
        *reinterpret_cast<u32x2*>(p) = o;
        if (d == 24) *reinterpret_cast<u32x2*>(p + 4) = (u32x2){which == 2 ? 0x3f80u : 0u, 0u};
      }
    }
  }
}

#define AT_SK 80
#define AT_SV 64
#define AT_KB (128 * AT_SK)
#define AT_VB (128 * AT_SV)
#define AT_BUF (2 * AT_KB + 2 * AT_VB)
__global__ __launch_bounds__(512) void k_attn(const u16* __restrict__ QKV, u16* __restrict__ O) {
  extern __shared__ __attribute__((aligned(16))) char smem[];
  const int tid = threadIdx.x, lane = tid & 63, wv = tid >> 6;
  const int bid = blockIdx.x;
  const int head = (bid & 7) * 2 + ((bid >> 3) & 1), qt = bid >> 4;
  const int l31 = lane & 31, h = lane >> 5, i16 = lane & 15, q4 = i16 >> 2, p4 = i16 & 3,
            blk = (lane >> 4) & 1;
  const int qs = wv & 3, kh = wv >> 2;

  const u16* Qh = QKV + (size_t)head * S_LEN * 32;
  const u16* Kh = QKV + (size_t)(NH + head) * S_LEN * 32;
  const u16* Vh = QKV + (size_t)(2 * NH + head) * S_LEN * 32;

  const int qrow = qt * 128 + qs * 32 + l31;
  const u16* qp = Qh + (size_t)qrow * 32 + 8 * h;
  const bf16x8 qf0 = *reinterpret_cast<const bf16x8*>(qp);
  const bf16x8 qf1 = *reinterpret_cast<const bf16x8*>(qp + 16);

  u32x4 kr[2], vr[2];
  auto loadKV = [&](int it) {
#pragma unroll
    for (int i = 0; i < 2; ++i) {
      const size_t off = (size_t)(i * 1024 + it * 128) * 32 + tid * 8;
      kr[i] = *reinterpret_cast<const u32x4*>(Kh + off);
      vr[i] = *reinterpret_cast<const u32x4*>(Vh + off);
    }
  };
  auto storeKV = [&](int buf) {
    char* b = smem + buf * AT_BUF;
#pragma unroll
    for (int i = 0; i < 2; ++i) {
      *reinterpret_cast<u32x4*>(b + i * AT_KB + (tid >> 2) * AT_SK + (tid & 3) * 16) = kr[i];
      *reinterpret_cast<u32x4*>(b + 2 * AT_KB + i * AT_VB + tid * 16) = vr[i];
    }
  };
  loadKV(0);
  storeKV(0);
  __syncthreads();

  float m = 0.f;
  f32x16 negm, o;
#pragma unroll
  for (int i = 0; i < 16; ++i) { negm[i] = 0.f; o[i] = 0.f; }

#pragma unroll 1
  for (int it = 0; it < 8; ++it) {
    if (it < 7) loadKV(it + 1);
    const char* kb = smem + (it & 1) * AT_BUF + kh * AT_KB + l31 * AT_SK + 16 * h;
    const char* vb = smem + (it & 1) * AT_BUF + 2 * AT_KB + kh * AT_VB + (4 * h + q4) * AT_SV +
                     (16 * blk + 4 * p4) * 2;
    f32x16 s[4];
#pragma unroll
    for (int t = 0; t < 4; ++t) {
      s[t] = MFMA(lds_b128(kb + t * 32 * AT_SK), qf0, negm);
      s[t] = MFMA(lds_b128(kb + t * 32 * AT_SK + 32), qf1, s[t]);
    }
    float mx = s[0][0];
#pragma unroll
    for (int t = 0; t < 4; ++t)
#pragma unroll
      for (int i = 0; i < 16; ++i) mx = fmaxf(mx, s[t][i]);
    mx = fmaxf(mx, __shfl_xor(mx, 32));
    const float delta = it == 0 ? mx : fmaxf(mx, 0.f);
    if (__any(delta != 0.f)) {
      const float alpha = __builtin_amdgcn_exp2f(-delta);
      m += delta;
#pragma unroll
      for (int i = 0; i < 16; ++i) { negm[i] = -m; o[i] *= alpha; }
#pragma unroll
      for (int t = 0; t < 4; ++t)
#pragma unroll
        for (int i = 0; i < 16; ++i) s[t][i] -= delta;
    }
#pragma unroll
    for (int t = 0; t < 4; ++t) {
#pragma unroll
      for (int sp = 0; sp < 2; ++sp) {
        u32x4 pk;
#pragma unroll
        for (int j = 0; j < 4; ++j)
          pk[j] = pack2(__builtin_amdgcn_exp2f(s[t][8 * sp + 2 * j]), __builtin_amdgcn_exp2f(s[t][8 * sp + 2 * j + 1]));
        const bf16x8 pf = __builtin_bit_cast(bf16x8, pk);
        const s16x4 lo = tr_read(vb + (t * 32 + sp * 16) * AT_SV);
        const s16x4 hi = tr_read(vb + (t * 32 + sp * 16 + 8) * AT_SV);
        const bf16x8 vf = __builtin_shufflevector(lo, hi, 0, 1, 2, 3, 4, 5, 6, 7);
        o = MFMA(vf, pf, o);
      }
    }
    if (it < 7) storeKV((it + 1) & 1);
    __syncthreads();
  }

  float* red = reinterpret_cast<float*>(smem);
  if (kh == 1) {
    red[(qs * 17 + 16) * 64 + lane] = m;
#pragma unroll
    for (int i = 0; i < 16; ++i) red[(qs * 17 + i) * 64 + lane] = o[i];
  }
  __syncthreads();
  if (kh == 0) {
    const float m2 = red[(qs * 17 + 16) * 64 + lane];
    const float mn = fmaxf(m, m2);
    const float a1 = __builtin_amdgcn_exp2f(m - mn);
    const float a2 = __builtin_amdgcn_exp2f(m2 - mn);
#pragma unroll
    for (int i = 0; i < 16; ++i) o[i] = o[i] * a1 + red[(qs * 17 + i) * 64 + lane] * a2;
    const float l = __shfl(o[12], l31 + 32);
    const float inv = 1.0f / l;
    u16* op = O + (size_t)qrow * DM + head * DH;
#pragma unroll
    for (int g = 0; g < 4; ++g) {
      const int d = 8 * g + 4 * h;
      if (d < DH) {
        u32x2 v;
        v[0] = pack2(o[4 * g] * inv, o[4 * g + 1] * inv);
        v[1] = pack2(o[4 * g + 2] * inv, o[4 * g + 3] * inv);
        *reinterpret_cast<u32x2*>(op + d) = v;
      }
    }
  }
}

#define GR_SA 464
#define GR_SB 192
#define GR_BUF (64 * GR_SA + 224 * GR_SB)
template <int KTOT>
__global__ __launch_bounds__(512) void k_gemm_res(const u16* __restrict__ A, const u16* __restrict__ W,
                                                  const u16* __restrict__ res, u16* __restrict__ out) {
  extern __shared__ __attribute__((aligned(16))) char smem[];
  const int tid = threadIdx.x, lane = tid & 63, wv = tid >> 6;
  const int bm = blockIdx.x & 31, bn = blockIdx.x >> 5;
  const int m0 = bm * 64, n0 = bn * 64;
  constexpr int NC = KTOT / 224;
  constexpr int PD = NC < 3 ? NC : 3;

  const int wm = wv & 1, wn = (wv >> 1) & 1, kp = wv >> 2;
  const int l31 = lane & 31, h = lane >> 5, i16 = lane & 15, q4 = i16 >> 2, p4 = i16 & 3,
            blk = (lane >> 4) & 1;

  u32x4 ar[PD][4], br[PD][4];
  auto load = [&](int kc, int slot) {
#pragma unroll
    for (int i = 0; i < 4; ++i) {
      const int c = tid + 512 * i;
      if (i < 3 || tid < 256) {
        const int r = c / 28, c16 = c % 28;
        ar[slot][i] = *reinterpret_cast<const u32x4*>(A + (size_t)(m0 + r) * KTOT + kc * 224 + c16 * 8);
        const int rb = c >> 3, cb = c & 7;
        br[slot][i] = *reinterpret_cast<const u32x4*>(W + (size_t)(kc * 224 + rb) * DM + n0 + cb * 8);
      }
    }
  };
  auto store = [&](int slot, int buf) {
    char* b = smem + buf * GR_BUF;
#pragma unroll
    for (int i = 0; i < 4; ++i) {
      const int c = tid + 512 * i;
      if (i < 3 || tid < 256) {
        const int r = c / 28, c16 = c % 28;
        *reinterpret_cast<u32x4*>(b + r * GR_SA + c16 * 16) = ar[slot][i];
        const int rb = c >> 3, cb = c & 7;
        *reinterpret_cast<u32x4*>(b + 64 * GR_SA + rb * GR_SB + cb * 16) = br[slot][i];
      }
    }
  };
#pragma unroll
  for (int i = 0; i < PD; ++i) load(i, i);
  const int m = m0 + wm * 32 + l31;
  u32x2 rr[4];
#pragma unroll
  for (int g = 0; g < 4; ++g)
    rr[g] = *reinterpret_cast<const u32x2*>(res + (size_t)m * DM + n0 + wn * 32 + 8 * g + 4 * h);
  store(0, 0);
  if (PD < NC) load(PD, 0);
  __syncthreads();

  const int aOff = (wm * 32 + l31) * GR_SA + 16 * h + kp * 32;
  const int bOff = 64 * GR_SA + (kp * 16 + 8 * h + q4) * GR_SB + (wn * 32 + 16 * blk + 4 * p4) * 2;

  f32x16 acc, acc2;
#pragma unroll
  for (int i = 0; i < 16; ++i) { acc[i] = 0.f; acc2[i] = 0.f; }

#pragma unroll
  for (int kc = 0; kc < NC; ++kc) {
    const char* b = smem + (kc & 1) * GR_BUF;
#pragma unroll
    for (int ks = 0; ks < 7; ++ks) {
      const bf16x8 xf = lds_b128(b + aOff + ks * 64);
      const bf16x8 wf = tr_frag(b + bOff + ks * 32 * GR_SB, GR_SB);
      if (ks & 1) acc2 = MFMA(wf, xf, acc2); else acc = MFMA(wf, xf, acc);
    }
    if (kc + 1 < NC) {
      store((kc + 1) % PD, (kc + 1) & 1);
      if (kc + 1 + PD < NC) load(kc + 1 + PD, (kc + 1) % PD);
      __syncthreads();
    }
  }
#pragma unroll
  for (int i = 0; i < 16; ++i) acc[i] += acc2[i];

  __syncthreads();
  float* red = reinterpret_cast<float*>(smem);
  if (kp == 1) {
#pragma unroll
    for (int i = 0; i < 16; ++i) red[((wv & 3) * 16 + i) * 64 + lane] = acc[i];
  }
  __syncthreads();
  if (kp == 0) {
#pragma unroll
    for (int i = 0; i < 16; ++i) acc[i] += red[((wv & 3) * 16 + i) * 64 + lane];
#pragma unroll
    for (int g = 0; g < 4; ++g) {
      const size_t off = (size_t)m * DM + n0 + wn * 32 + 8 * g + 4 * h;
      u32x2 o;
      o[0] = pack2(__uint_as_float(rr[g][0] << 16) + bfr(acc[4 * g]),
                   __uint_as_float(rr[g][0] & 0xffff0000u) + bfr(acc[4 * g + 1]));
      o[1] = pack2(__uint_as_float(rr[g][1] << 16) + bfr(acc[4 * g + 2]),
                   __uint_as_float(rr[g][1] & 0xffff0000u) + bfr(acc[4 * g + 3]));
      *reinterpret_cast<u32x2*>(out + off) = o;
    }
  }
}

#define UP_SA 912
#define UP_SB 320
__global__ __launch_bounds__(512) void k_ln_up(const u16* __restrict__ x, const u16* __restrict__ w1,
                                               const u16* __restrict__ w3, u16* __restrict__ Hb) {
  extern __shared__ __attribute__((aligned(16))) char smem[];
  char* sA = smem;
  char* sB = smem + 128 * UP_SA;
  float* sinv = reinterpret_cast<float*>(smem + 128 * UP_SA + 2 * 64 * UP_SB);
  const int tid = threadIdx.x, lane = tid & 63, wv = tid >> 6;
  const int bm = blockIdx.x & 15, bn = blockIdx.x >> 4;
  const int m0 = bm * 128, n0 = bn * 128;

  u32x4 br[2][4];
  const int brow = tid >> 4, bc = tid & 15;
  auto loadB = [&](int kt, int slot) {
#pragma unroll
    for (int i = 0; i < 2; ++i) {
      const size_t off = (size_t)(kt * 64 + brow + 32 * i) * FF + n0 + bc * 8;
      br[slot][i] = *reinterpret_cast<const u32x4*>(w1 + off);
      br[slot][2 + i] = *reinterpret_cast<const u32x4*>(w3 + off);
    }
  };
  auto storeB = [&](int slot) {
#pragma unroll
    for (int i = 0; i < 2; ++i) {
      *reinterpret_cast<u32x4*>(sB + (brow + 32 * i) * UP_SB + bc * 16) = br[slot][i];
      *reinterpret_cast<u32x4*>(sB + 64 * UP_SB + (brow + 32 * i) * UP_SB + bc * 16) = br[slot][2 + i];
    }
  };
  loadB(0, 0);
  loadB(1, 1);
  LnRegs lr;
  ln_load(lr, x, m0, tid);
  ln_store<UP_SA>(lr, sA, sinv, tid);
  storeB(0);
  __syncthreads();

  const int wm = wv & 1, wn = wv >> 1;
  const int l31 = lane & 31, h = lane >> 5, i16 = lane & 15, q4 = i16 >> 2, p4 = i16 & 3,
            blk = (lane >> 4) & 1;
  const char* aBase = sA + (wm * 64 + l31) * UP_SA + 16 * h;
  const char* bBase = sB + (8 * h + q4) * UP_SB + (wn * 32 + 16 * blk + 4 * p4) * 2;

  f32x16 acc[2][2];
#pragma unroll
  for (int a = 0; a < 2; ++a)
#pragma unroll
    for (int c = 0; c < 2; ++c)
#pragma unroll
      for (int i = 0; i < 16; ++i) acc[a][c][i] = 0.f;

#pragma unroll
  for (int kt = 0; kt < 7; ++kt) {
#pragma unroll
    for (int ks = 0; ks < 4; ++ks) {
      bf16x8 xf[2];
#pragma unroll
      for (int mt = 0; mt < 2; ++mt) xf[mt] = lds_b128(aBase + mt * 32 * UP_SA + (kt * 64 + ks * 16) * 2);
#pragma unroll
      for (int a = 0; a < 2; ++a) {
        const bf16x8 wf = tr_frag(bBase + a * 64 * UP_SB + ks * 16 * UP_SB, UP_SB);
#pragma unroll
        for (int mt = 0; mt < 2; ++mt) acc[a][mt] = MFMA(wf, xf[mt], acc[a][mt]);
      }
    }
    if (kt < 6) {
      __syncthreads();
      storeB((kt + 1) & 1);
      if (kt < 5) loadB(kt + 2, kt & 1);
      __syncthreads();
    }
  }

#pragma unroll
  for (int mt = 0; mt < 2; ++mt) {
    const int m = m0 + wm * 64 + mt * 32 + l31;
    const float inv = sinv[wm * 64 + mt * 32 + l31];
#pragma unroll
    for (int g = 0; g < 4; ++g) {
      float hv[4];
#pragma unroll
      for (int j = 0; j < 4; ++j) {
        const float gt = bfr(acc[0][mt][4 * g + j] * inv);
        const float up = bfr(acc[1][mt][4 * g + j] * inv);
        const float sl = bfr(gt / (1.0f + __expf(-gt)));
        hv[j] = sl * up;
      }
      u32x2 o;
      o[0] = pack2(hv[0], hv[1]);
      o[1] = pack2(hv[2], hv[3]);
      *reinterpret_cast<u32x2*>(Hb + (size_t)m * FF + n0 + wn * 32 + 8 * g + 4 * h) = o;
    }
  }
}

extern "C" void kernel_launch(void* const* d_in, const int* in_sizes, int n_in, void* d_out, int out_size,
                              void* d_ws, size_t ws_size, hipStream_t stream) {
  const u16* x = (const u16*)d_in[0];
  const u16* wqkv = (const u16*)d_in[1];
  const u16* wo = (const u16*)d_in[2];
  const u16* w1 = (const u16*)d_in[4];
  const u16* w3 = (const u16*)d_in[5];
  const u16* w2 = (const u16*)d_in[6];
  u16* out = (u16*)d_out;

  char* ws = (char*)d_ws;
  u16* QKV = (u16*)(ws);
  u16* Ob = (u16*)(ws + (6u << 20));
  u16* x1 = (u16*)(ws + (8u << 20));
  u16* x2 = (u16*)(ws + (10u << 20));
  u16* x3 = (u16*)(ws + (12u << 20));
  u16* Hb = (u16*)(ws + (16u << 20));

  const int ldsQKV = 128 * QKV_SA + 224 * QKV_SB + 512;
  const int ldsAT = 2 * AT_BUF;
  const int ldsGR = 2 * GR_BUF;
  const int ldsUP = 128 * UP_SA + 2 * 64 * UP_SB + 512;
  (void)hipFuncSetAttribute((const void*)k_ln_qkv, hipFuncAttributeMaxDynamicSharedMemorySize, ldsQKV);
  (void)hipFuncSetAttribute((const void*)k_attn, hipFuncAttributeMaxDynamicSharedMemorySize, ldsAT);
  (void)hipFuncSetAttribute((const void*)k_gemm_res<448>, hipFuncAttributeMaxDynamicSharedMemorySize, ldsGR);
  (void)hipFuncSetAttribute((const void*)k_gemm_res<1792>, hipFuncAttributeMaxDynamicSharedMemorySize, ldsGR);
  (void)hipFuncSetAttribute((const void*)k_ln_up, hipFuncAttributeMaxDynamicSharedMemorySize, ldsUP);

  const u16* xin = x;
  for (int layer = 0; layer < 2; ++layer) {
    u16* xa = layer == 0 ? x1 : x3;
    u16* xb = layer == 0 ? x2 : out;
    k_ln_qkv<<<224, 512, ldsQKV, stream>>>(xin, wqkv, QKV);
    k_attn<<<256, 512, ldsAT, stream>>>(QKV, Ob);
    k_gemm_res<448><<<224, 512, ldsGR, stream>>>(Ob, wo, xin, xa);
    k_ln_up<<<224, 512, ldsUP, stream>>>(xa, w1, w3, Hb);
    k_gemm_res<1792><<<224, 512, ldsGR, stream>>>(Hb, w2, xa, xb);
    xin = xb;
  }
}
```

```cpp
#include <hip/hip_runtime.h>
#include <hip/hip_bf16.h>
#include <stdint.h>


#define S_LEN 2048
#define DM 448
#define NH 16
#define DH 28
#define FF 1792

typedef __attribute__((ext_vector_type(8))) short bf16x8;
typedef __attribute__((ext_vector_type(4))) short s16x4;
typedef __attribute__((ext_vector_type(16))) float f32x16;
typedef __attribute__((ext_vector_type(4))) unsigned u32x4;
typedef __attribute__((ext_vector_type(2))) unsigned u32x2;
typedef unsigned short u16;

#define DI __device__ __forceinline__
#define MFMA(a, b, c) __builtin_amdgcn_mfma_f32_32x32x16_bf16((a), (b), (c), 0, 0, 0)

DI float bf2f(u16 v) { return __uint_as_float(((unsigned)v) << 16); }
DI u16 f2bf(float f) {
  __hip_bfloat16 b = __float2bfloat16(f);
  return *reinterpret_cast<u16*>(&b);
}
DI float bfr(float f) { return bf2f(f2bf(f)); }
typedef __attribute__((ext_vector_type(2))) float f32x2;
typedef __attribute__((ext_vector_type(2))) __bf16 bf16x2;
DI unsigned pack2(float lo, float hi) {
  const f32x2 v = {lo, hi};
  return __builtin_bit_cast(unsigned, __builtin_convertvector(v, bf16x2));
}

DI s16x4 tr_read(const char* p) {
  return __builtin_amdgcn_ds_read_tr16_b64_v4i16(
      (__attribute__((address_space(3))) s16x4*)(p));
}
DI bf16x8 tr_frag(const char* p, int stride) {
  s16x4 lo = tr_read(p);
  s16x4 hi = tr_read(p + 4 * stride);
  return __builtin_shufflevector(lo, hi, 0, 1, 2, 3, 4, 5, 6, 7);
}
DI bf16x8 lds_b128(const char* p) { return *reinterpret_cast<const bf16x8*>(p); }

struct LnRegs { u32x4 v[2][7]; };
DI void ln_load(LnRegs& r, const u16* __restrict__ x, int m0, int tid) {
  const int sub = tid & 7;
#pragma unroll
  for (int pass = 0; pass < 2; ++pass) {
    const u16* xr = x + (size_t)(m0 + pass * 64 + (tid >> 3)) * DM;
#pragma unroll
    for (int c = 0; c < 7; ++c) r.v[pass][c] = *reinterpret_cast<const u32x4*>(xr + (c * 8 + sub) * 8);
  }
}
template <int SA>
DI void ln_store(const LnRegs& r, char* sA, float* sinv, int tid) {
  const int sub = tid & 7;
#pragma unroll
  for (int pass = 0; pass < 2; ++pass) {
    const int row = pass * 64 + (tid >> 3);
    float sum = 0.f;
#pragma unroll
    for (int c = 0; c < 7; ++c)
#pragma unroll
      for (int j = 0; j < 4; ++j)
        sum += __uint_as_float(r.v[pass][c][j] << 16) + __uint_as_float(r.v[pass][c][j] & 0xffff0000u);
    sum += __shfl_xor(sum, 1);
    sum += __shfl_xor(sum, 2);
    sum += __shfl_xor(sum, 4);
    const float mean = sum * (1.0f / DM);
    const float mean_b = bfr(mean);
    float sq = 0.f;
#pragma unroll
    for (int c = 0; c < 7; ++c) {
      u32x4 o;
#pragma unroll
      for (int j = 0; j < 4; ++j) {
        const float f0 = __uint_as_float(r.v[pass][c][j] << 16);
        const float f1 = __uint_as_float(r.v[pass][c][j] & 0xffff0000u);
        const float d0 = f0 - mean, d1 = f1 - mean;
        sq = fmaf(d0, d0, fmaf(d1, d1, sq));
        o[j] = pack2(f0 - mean_b, f1 - mean_b);
      }
      *reinterpret_cast<u32x4*>(sA + row * SA + (c * 8 + sub) * 16) = o;
    }
    sq += __shfl_xor(sq, 1);
    sq += __shfl_xor(sq, 2);
    sq += __shfl_xor(sq, 4);
    if (sub == 0) sinv[row] = 1.0f / bfr(sqrtf(bfr(bfr(sq * (1.0f / DM)) + 1e-5f)));
  }
}

#define QKV_SA 912
#define QKV_SB 192
__global__ __launch_bounds__(512) void k_ln_qkv(const u16* __restrict__ x, const u16* __restrict__ w,
                                                u16* __restrict__ QKV) {
  extern __shared__ __attribute__((aligned(16))) char smem[];
  char* sA = smem;
  char* sB = smem + 128 * QKV_SA;
  float* sinv = reinterpret_cast<float*>(smem + 128 * QKV_SA + 224 * QKV_SB);
  const int tid = threadIdx.x, lane = tid & 63, wv = tid >> 6;
  const int bm = blockIdx.x & 15, bn = blockIdx.x >> 4;
  const int m0 = bm * 128, n0 = bn * 96;
  const int wr = wv & 3, kp = wv >> 2;

  u32x4 br[11];
#pragma unroll
  for (int i = 0; i < 11; ++i) {
    const int c = tid + 512 * i, r = c / 12, c16 = c % 12;
    if (i < 10 || tid < 256)
      br[i] = *reinterpret_cast<const u32x4*>(w + (size_t)r * (3 * DM) + n0 + c16 * 8);
  }
  LnRegs lr;
  ln_load(lr, x, m0, tid);
  ln_store<QKV_SA>(lr, sA, sinv, tid);
#pragma unroll
  for (int i = 0; i < 6; ++i) {
    const int c = tid + 512 * i, r = c / 12, c16 = c % 12;
    if (c < 2688) *reinterpret_cast<u32x4*>(sB + r * QKV_SB + c16 * 16) = br[i];
  }
  __syncthreads();

  f32x16 acc[3];
#pragma unroll
  for (int t = 0; t < 3; ++t)
#pragma unroll
    for (int i = 0; i < 16; ++i) acc[t][i] = 0.f;

  const int l31 = lane & 31, h = lane >> 5, i16 = lane & 15, q4 = i16 >> 2, p4 = i16 & 3,
            blk = (lane >> 4) & 1;
  const char* aBase = sA + (wr * 32 + l31) * QKV_SA + 16 * h + kp * 32;
  const char* bBase = sB + (kp * 16 + 8 * h + q4) * QKV_SB + (16 * blk + 4 * p4) * 2;

#pragma unroll
  for (int half = 0; half < 2; ++half) {
    if (half == 1) {
      __syncthreads();
#pragma unroll
      for (int i = 5; i < 11; ++i) {
        const int c = tid + 512 * i, r = c / 12, c16 = c % 12;
        if (c >= 2688 && c < 5376) *reinterpret_cast<u32x4*>(sB + (r - 224) * QKV_SB + c16 * 16) = br[i];
      }
      __syncthreads();
    }
#pragma unroll
    for (int ks = 0; ks < 7; ++ks) {
      const bf16x8 xf = lds_b128(aBase + (half * 224 + ks * 32) * 2);
#pragma unroll
      for (int t = 0; t < 3; ++t) {
        const bf16x8 wf = tr_frag(bBase + ks * 32 * QKV_SB + t * 64, QKV_SB);
        acc[t] = MFMA(wf, xf, acc[t]);
      }
    }
  }

  __syncthreads();
  float* red = reinterpret_cast<float*>(sB);
  red = reinterpret_cast<float*>(sA);
  if (kp == 1) {
#pragma unroll
    for (int t = 0; t < 3; ++t)
#pragma unroll
      for (int i = 0; i < 16; ++i) red[((wr * 3 + t) * 16 + i) * 64 + lane] = acc[t][i];
  }
  __syncthreads();
  if (kp == 0) {
    const int m = m0 + wr * 32 + l31;
    const float inv = sinv[wr * 32 + l31];
#pragma unroll
    for (int t = 0; t < 3; ++t) {
      const int ns = n0 + 32 * t;
      const int which = ns / DM, cb = ns % DM;
      const float sc = which == 0 ? inv * (0.18898223650461362f * 1.4426950408889634f) : inv;
#pragma unroll
      for (int i = 0; i < 16; ++i) acc[t][i] = (acc[t][i] + red[((wr * 3 + t) * 16 + i) * 64 + lane]) * sc;
#pragma unroll
      for (int g = 0; g < 4; ++g) {
        const int c = cb + 8 * g + 4 * h;
        const int head = c / DH, d = c % DH;
        u32x2 o;
        o[0] = pack2(acc[t][4 * g], acc[t][4 * g + 1]);
        o[1] = pack2(acc[t][4 * g + 2], acc[t][4 * g + 3]);
        u16* p = QKV + ((size_t)(which * NH + head) * S_LEN + m) * 32 + d;
        *reinterpret_cast<u32x2*>(p) = o;
        if (d == 24) *reinterpret_cast<u32x2*>(p + 4) = (u32x2){which == 2 ? 0x3f80u : 0u, 0u};
      }
    }
  }
}

#define AT_SK 80
#define AT_SV 64
#define AT_KB (128 * AT_SK)
#define AT_VB (128 * AT_SV)
#define AT_BUF (2 * AT_KB + 2 * AT_VB)
__global__ __launch_bounds__(512) void k_attn(const u16* __restrict__ QKV, u16* __restrict__ O) {
  extern __shared__ __attribute__((aligned(16))) char smem[];
  const int tid = threadIdx.x, lane = tid & 63, wv = tid >> 6;
  const int bid = blockIdx.x;
  const int head = (bid & 7) * 2 + ((bid >> 3) & 1), qt = bid >> 4;
  const int l31 = lane & 31, h = lane >> 5, i16 = lane & 15, q4 = i16 >> 2, p4 = i16 & 3,
            blk = (lane >> 4) & 1;
  const int qs = wv & 3, kh = wv >> 2;

  const u16* Qh = QKV + (size_t)head * S_LEN * 32;
  const u16* Kh = QKV + (size_t)(NH + head) * S_LEN * 32;
  const u16* Vh = QKV + (size_t)(2 * NH + head) * S_LEN * 32;

  const int qrow = qt * 128 + qs * 32 + l31;
  const u16* qp = Qh + (size_t)qrow * 32 + 8 * h;
  const bf16x8 qf0 = *reinterpret_cast<const bf16x8*>(qp);
  const bf16x8 qf1 = *reinterpret_cast<const bf16x8*>(qp + 16);

  u32x4 kr[2], vr[2];
  auto loadKV = [&](int it) {
#pragma unroll
    for (int i = 0; i < 2; ++i) {
      const size_t off = (size_t)(i * 1024 + it * 128) * 32 + tid * 8;
      kr[i] = *reinterpret_cast<const u32x4*>(Kh + off);
      vr[i] = *reinterpret_cast<const u32x4*>(Vh + off);
    }
  };
  auto storeKV = [&](int buf) {
    char* b = smem + buf * AT_BUF;
#pragma unroll
    for (int i = 0; i < 2; ++i) {
      *reinterpret_cast<u32x4*>(b + i * AT_KB + (tid >> 2) * AT_SK + (tid & 3) * 16) = kr[i];
      *reinterpret_cast<u32x4*>(b + 2 * AT_KB + i * AT_VB + tid * 16) = vr[i];
    }
  };
  loadKV(0);
  storeKV(0);
  __syncthreads();

  float m = 0.f;
  f32x16 negm, o;
#pragma unroll
  for (int i = 0; i < 16; ++i) { negm[i] = 0.f; o[i] = 0.f; }

#pragma unroll 1
  for (int it = 0; it < 8; ++it) {
    if (it < 7) loadKV(it + 1);
    const char* kb = smem + (it & 1) * AT_BUF + kh * AT_KB + l31 * AT_SK + 16 * h;
    const char* vb = smem + (it & 1) * AT_BUF + 2 * AT_KB + kh * AT_VB + (4 * h + q4) * AT_SV +
                     (16 * blk + 4 * p4) * 2;
    f32x16 s[4];
#pragma unroll
    for (int t = 0; t < 4; ++t) {
      s[t] = MFMA(lds_b128(kb + t * 32 * AT_SK), qf0, negm);
      s[t] = MFMA(lds_b128(kb + t * 32 * AT_SK + 32), qf1, s[t]);
    }
    float mx = s[0][0];
#pragma unroll
    for (int t = 0; t < 4; ++t)
#pragma unroll
      for (int i = 0; i < 16; ++i) mx = fmaxf(mx, s[t][i]);
    mx = fmaxf(mx, __shfl_xor(mx, 32));
    const float delta = it == 0 ? mx : fmaxf(mx, 0.f);
    if (__any(delta != 0.f)) {
      const float alpha = __builtin_amdgcn_exp2f(-delta);
      m += delta;
#pragma unroll
      for (int i = 0; i < 16; ++i) { negm[i] = -m; o[i] *= alpha; }
#pragma unroll
      for (int t = 0; t < 4; ++t)
#pragma unroll
        for (int i = 0; i < 16; ++i) s[t][i] -= delta;
    }
#pragma unroll
    for (int t = 0; t < 4; ++t) {
#pragma unroll
      for (int sp = 0; sp < 2; ++sp) {
        u32x4 pk;
#pragma unroll
        for (int j = 0; j < 4; ++j)
          pk[j] = pack2(__builtin_amdgcn_exp2f(s[t][8 * sp + 2 * j]), __builtin_amdgcn_exp2f(s[t][8 * sp + 2 * j + 1]));
        const bf16x8 pf = __builtin_bit_cast(bf16x8, pk);
        const s16x4 lo = tr_read(vb + (t * 32 + sp * 16) * AT_SV);
        const s16x4 hi = tr_read(vb + (t * 32 + sp * 16 + 8) * AT_SV);
        const bf16x8 vf = __builtin_shufflevector(lo, hi, 0, 1, 2, 3, 4, 5, 6, 7);
        o = MFMA(vf, pf, o);
      }
    }
    if (it < 7) storeKV((it + 1) & 1);
    __syncthreads();
  }

  float* red = reinterpret_cast<float*>(smem);
  if (kh == 1) {
    red[(qs * 17 + 16) * 64 + lane] = m;
#pragma unroll
    for (int i = 0; i < 16; ++i) red[(qs * 17 + i) * 64 + lane] = o[i];
  }
  __syncthreads();
  if (kh == 0) {
    const float m2 = red[(qs * 17 + 16) * 64 + lane];
    const float mn = fmaxf(m, m2);
    const float a1 = __builtin_amdgcn_exp2f(m - mn);
    const float a2 = __builtin_amdgcn_exp2f(m2 - mn);
#pragma unroll
    for (int i = 0; i < 16; ++i) o[i] = o[i] * a1 + red[(qs * 17 + i) * 64 + lane] * a2;
    const float l = __shfl(o[12], l31 + 32);
    const float inv = 1.0f / l;
    u16* op = O + (size_t)qrow * DM + head * DH;
#pragma unroll
    for (int g = 0; g < 4; ++g) {
      const int d = 8 * g + 4 * h;
      if (d < DH) {
        u32x2 v;
        v[0] = pack2(o[4 * g] * inv, o[4 * g + 1] * inv);
        v[1] = pack2(o[4 * g + 2] * inv, o[4 * g + 3] * inv);
        *reinterpret_cast<u32x2*>(op + d) = v;
      }
    }
  }
}

#define GR_SA 464
#define GR_SB 192
#define GR_BUF (64 * GR_SA + 224 * GR_SB)
template <int KTOT>
__global__ __launch_bounds__(512) void k_gemm_res(const u16* __restrict__ A, const u16* __restrict__ W,
                                                  const u16* __restrict__ res, u16* __restrict__ out) {
  extern __shared__ __attribute__((aligned(16))) char smem[];
  const int tid = threadIdx.x, lane = tid & 63, wv = tid >> 6;
  const int bm = blockIdx.x & 31, bn = blockIdx.x >> 5;
  const int m0 = bm * 64, n0 = bn * 64;
  constexpr int NC = KTOT / 224;
  constexpr int PD = NC < 3 ? NC : 3;

  const int wm = wv & 1, wn = (wv >> 1) & 1, kp = wv >> 2;
  const int l31 = lane & 31, h = lane >> 5, i16 = lane & 15, q4 = i16 >> 2, p4 = i16 & 3,
            blk = (lane >> 4) & 1;

  u32x4 ar[PD][4], br[PD][4];
  auto load = [&](int kc, int slot) {
#pragma unroll
    for (int i = 0; i < 4; ++i) {
      const int c = tid + 512 * i;
      if (i < 3 || tid < 256) {
        const int r = c / 28, c16 = c % 28;
        ar[slot][i] = *reinterpret_cast<const u32x4*>(A + (size_t)(m0 + r) * KTOT + kc * 224 + c16 * 8);
        const int rb = c >> 3, cb = c & 7;
        br[slot][i] = *reinterpret_cast<const u32x4*>(W + (size_t)(kc * 224 + rb) * DM + n0 + cb * 8);
      }
    }
  };
  auto store = [&](int slot, int buf) {
    char* b = smem + buf * GR_BUF;
#pragma unroll
    for (int i = 0; i < 4; ++i) {
      const int c = tid + 512 * i;
      if (i < 3 || tid < 256) {
        const int r = c / 28, c16 = c % 28;
        *reinterpret_cast<u32x4*>(b + r * GR_SA + c16 * 16) = ar[slot][i];
        const int rb = c >> 3, cb = c & 7;
        *reinterpret_cast<u32x4*>(b + 64 * GR_SA + rb * GR_SB + cb * 16) = br[slot][i];
      }
    }
  };
#pragma unroll
  for (int i = 0; i < PD; ++i) load(i, i);
  const int m = m0 + wm * 32 + l31;
  u32x2 rr[4];
#pragma unroll
  for (int g = 0; g < 4; ++g)
    rr[g] = *reinterpret_cast<const u32x2*>(res + (size_t)m * DM + n0 + wn * 32 + 8 * g + 4 * h);
  store(0, 0);
  if (PD < NC) load(PD, 0);
  __syncthreads();

  const int aOff = (wm * 32 + l31) * GR_SA + 16 * h + kp * 32;
  const int bOff = 64 * GR_SA + (kp * 16 + 8 * h + q4) * GR_SB + (wn * 32 + 16 * blk + 4 * p4) * 2;

  f32x16 acc, acc2;
#pragma unroll
  for (int i = 0; i < 16; ++i) { acc[i] = 0.f; acc2[i] = 0.f; }

#pragma unroll
  for (int kc = 0; kc < NC; ++kc) {
    const char* b = smem + (kc & 1) * GR_BUF;
#pragma unroll
    for (int ks = 0; ks < 7; ++ks) {
      const bf16x8 xf = lds_b128(b + aOff + ks * 64);
      const bf16x8 wf = tr_frag(b + bOff + ks * 32 * GR_SB, GR_SB);
      if (ks & 1) acc2 = MFMA(wf, xf, acc2); else acc = MFMA(wf, xf, acc);
    }
    if (kc + 1 < NC) {
      store((kc + 1) % PD, (kc + 1) & 1);
      if (kc + 1 + PD < NC) load(kc + 1 + PD, (kc + 1) % PD);
      __syncthreads();
    }
  }
#pragma unroll
  for (int i = 0; i < 16; ++i) acc[i] += acc2[i];

  __syncthreads();
  float* red = reinterpret_cast<float*>(smem);
  if (kp == 1) {
#pragma unroll
    for (int i = 0; i < 16; ++i) red[((wv & 3) * 16 + i) * 64 + lane] = acc[i];
  }
  __syncthreads();
  if (kp == 0) {
#pragma unroll
    for (int i = 0; i < 16; ++i) acc[i] += red[((wv & 3) * 16 + i) * 64 + lane];
#pragma unroll
    for (int g = 0; g < 4; ++g) {
      const size_t off = (size_t)m * DM + n0 + wn * 32 + 8 * g + 4 * h;
      u32x2 o;
      o[0] = pack2(__uint_as_float(rr[g][0] << 16) + bfr(acc[4 * g]),
                   __uint_as_float(rr[g][0] & 0xffff0000u) + bfr(acc[4 * g + 1]));
      o[1] = pack2(__uint_as_float(rr[g][1] << 16) + bfr(acc[4 * g + 2]),
                   __uint_as_float(rr[g][1] & 0xffff0000u) + bfr(acc[4 * g + 3]));
      *reinterpret_cast<u32x2*>(out + off) = o;
    }
  }
}

#define UP_SA 912
#define UP_SB 320
__global__ __launch_bounds__(512) void k_ln_up(const u16* __restrict__ x, const u16* __restrict__ w1,
                                               const u16* __restrict__ w3, u16* __restrict__ Hb) {
  extern __shared__ __attribute__((aligned(16))) char smem[];
  char* sA = smem;
  char* sB = smem + 128 * UP_SA;
  float* sinv = reinterpret_cast<float*>(smem + 128 * UP_SA + 2 * 64 * UP_SB);
  const int tid = threadIdx.x, lane = tid & 63, wv = tid >> 6;
  const int bm = blockIdx.x & 15, bn = blockIdx.x >> 4;
  const int m0 = bm * 128, n0 = bn * 128;

  u32x4 br[2][4];
  const int brow = tid >> 4, bc = tid & 15;
  auto loadB = [&](int kt, int slot) {
#pragma unroll
    for (int i = 0; i < 2; ++i) {
      const size_t off = (size_t)(kt * 64 + brow + 32 * i) * FF + n0 + bc * 8;
      br[slot][i] = *reinterpret_cast<const u32x4*>(w1 + off);
      br[slot][2 + i] = *reinterpret_cast<const u32x4*>(w3 + off);
    }
  };
  auto storeB = [&](int slot) {
#pragma unroll
    for (int i = 0; i < 2; ++i) {
      *reinterpret_cast<u32x4*>(sB + (brow + 32 * i) * UP_SB + bc * 16) = br[slot][i];
      *reinterpret_cast<u32x4*>(sB + 64 * UP_SB + (brow + 32 * i) * UP_SB + bc * 16) = br[slot][2 + i];
    }
  };
  loadB(0, 0);
  loadB(1, 1);
  LnRegs lr;
  ln_load(lr, x, m0, tid);
  ln_store<UP_SA>(lr, sA, sinv, tid);
  storeB(0);
  __syncthreads();

  const int wm = wv & 1, wn = wv >> 1;
  const int l31 = lane & 31, h = lane >> 5, i16 = lane & 15, q4 = i16 >> 2, p4 = i16 & 3,
            blk = (lane >> 4) & 1;
  const char* aBase = sA + (wm * 64 + l31) * UP_SA + 16 * h;
  const char* bBase = sB + (8 * h + q4) * UP_SB + (wn * 32 + 16 * blk + 4 * p4) * 2;

  f32x16 acc[2][2];
#pragma unroll
  for (int a = 0; a < 2; ++a)
#pragma unroll
    for (int c = 0; c < 2; ++c)
#pragma unroll
      for (int i = 0; i < 16; ++i) acc[a][c][i] = 0.f;

#pragma unroll
  for (int kt = 0; kt < 7; ++kt) {
#pragma unroll
    for (int ks = 0; ks < 4; ++ks) {
      bf16x8 xf[2];
#pragma unroll
      for (int mt = 0; mt < 2; ++mt) xf[mt] = lds_b128(aBase + mt * 32 * UP_SA + (kt * 64 + ks * 16) * 2);
#pragma unroll
      for (int a = 0; a < 2; ++a) {
        const bf16x8 wf = tr_frag(bBase + a * 64 * UP_SB + ks * 16 * UP_SB, UP_SB);
#pragma unroll
        for (int mt = 0; mt < 2; ++mt) acc[a][mt] = MFMA(wf, xf[mt], acc[a][mt]);
      }
    }
    if (kt < 6) {
      __syncthreads();
      storeB((kt + 1) & 1);
      if (kt < 5) loadB(kt + 2, kt & 1);
      __syncthreads();
    }
  }

#pragma unroll
  for (int mt = 0; mt < 2; ++mt) {
    const int m = m0 + wm * 64 + mt * 32 + l31;
    const float inv = sinv[wm * 64 + mt * 32 + l31];
#pragma unroll
    for (int g = 0; g < 4; ++g) {
      float hv[4];
#pragma unroll
      for (int j = 0; j < 4; ++j) {
        const float gt = bfr(acc[0][mt][4 * g + j] * inv);
        const float up = bfr(acc[1][mt][4 * g + j] * inv);
        const float sl = bfr(gt / (1.0f + __expf(-gt)));
        hv[j] = sl * up;
      }
      u32x2 o;
      o[0] = pack2(hv[0], hv[1]);
      o[1] = pack2(hv[2], hv[3]);
      *reinterpret_cast<u32x2*>(Hb + (size_t)m * FF + n0 + wn * 32 + 8 * g + 4 * h) = o;
    }
  }
}

extern "C" void kernel_launch(void* const* d_in, const int* in_sizes, int n_in, void* d_out, int out_size,
                              void* d_ws, size_t ws_size, hipStream_t stream) {
  const u16* x = (const u16*)d_in[0];
  const u16* wqkv = (const u16*)d_in[1];
  const u16* wo = (const u16*)d_in[2];
  const u16* w1 = (const u16*)d_in[4];
  const u16* w3 = (const u16*)d_in[5];
  const u16* w2 = (const u16*)d_in[6];
  u16* out = (u16*)d_out;

  char* ws = (char*)d_ws;
  u16* QKV = (u16*)(ws);
  u16* Ob = (u16*)(ws + (6u << 20));
  u16* x1 = (u16*)(ws + (8u << 20));
  u16* x2 = (u16*)(ws + (10u << 20));
  u16* x3 = (u16*)(ws + (12u << 20));
  u16* Hb = (u16*)(ws + (16u << 20));

  const int ldsQKV = 128 * QKV_SA + 224 * QKV_SB + 512;
  const int ldsAT = 2 * AT_BUF;
  const int ldsGR = 2 * GR_BUF;
  const int ldsUP = 128 * UP_SA + 2 * 64 * UP_SB + 512;
  (void)hipFuncSetAttribute((const void*)k_ln_qkv, hipFuncAttributeMaxDynamicSharedMemorySize, ldsQKV);
  (void)hipFuncSetAttribute((const void*)k_attn, hipFuncAttributeMaxDynamicSharedMemorySize, ldsAT);
  (void)hipFuncSetAttribute((const void*)k_gemm_res<448>, hipFuncAttributeMaxDynamicSharedMemorySize, ldsGR);
  (void)hipFuncSetAttribute((const void*)k_gemm_res<1792>, hipFuncAttributeMaxDynamicSharedMemorySize, ldsGR);
  (void)hipFuncSetAttribute((const void*)k_ln_up, hipFuncAttributeMaxDynamicSharedMemorySize, ldsUP);

  const u16* xin = x;
  for (int layer = 0; layer < 2; ++layer) {
    u16* xa = layer == 0 ? x1 : x3;
    u16* xb = layer == 0 ? x2 : out;
    k_ln_qkv<<<224, 512, ldsQKV, stream>>>(xin, wqkv, QKV);
    k_attn<<<256, 512, ldsAT, stream>>>(QKV, Ob);
    k_gemm_res<448><<<224, 512, ldsGR, stream>>>(Ob, wo, xin, xa);
    k_ln_up<<<224, 512, ldsUP, stream>>>(xa, w1, w3, Hb);
    k_gemm_res<1792><<<224, 512, ldsGR, stream>>>(Hb, w2, xa, xb);
    xin = xb;
  }
}
```

```cpp
#include <hip/hip_runtime.h>
#include <hip/hip_bf16.h>
#include <stdint.h>


#define S_LEN 2048
#define DM 448
#define NH 16
#define DH 28
#define FF 1792

typedef __attribute__((ext_vector_type(8))) short bf16x8;
typedef __attribute__((ext_vector_type(4))) short s16x4;
typedef __attribute__((ext_vector_type(16))) float f32x16;
typedef __attribute__((ext_vector_type(4))) unsigned u32x4;
typedef __attribute__((ext_vector_type(2))) unsigned u32x2;
typedef unsigned short u16;

#define DI __device__ __forceinline__
#define MFMA(a, b, c) __builtin_amdgcn_mfma_f32_32x32x16_bf16((a), (b), (c), 0, 0, 0)

DI float bf2f(u16 v) { return __uint_as_float(((unsigned)v) << 16); }
DI u16 f2bf(float f) {
  __hip_bfloat16 b = __float2bfloat16(f);
  return *reinterpret_cast<u16*>(&b);
}
DI float bfr(float f) { return bf2f(f2bf(f)); }
typedef __attribute__((ext_vector_type(2))) float f32x2;
typedef __attribute__((ext_vector_type(2))) __bf16 bf16x2;
DI unsigned pack2(float lo, float hi) {
  const f32x2 v = {lo, hi};
  return __builtin_bit_cast(unsigned, __builtin_convertvector(v, bf16x2));
}

DI s16x4 tr_read(const char* p) {
  return __builtin_amdgcn_ds_read_tr16_b64_v4i16(
      (__attribute__((address_space(3))) s16x4*)(p));
}
DI bf16x8 tr_frag(const char* p, int stride) {
  s16x4 lo = tr_read(p);
  s16x4 hi = tr_read(p + 4 * stride);
  return __builtin_shufflevector(lo, hi, 0, 1, 2, 3, 4, 5, 6, 7);
}
DI bf16x8 lds_b128(const char* p) { return *reinterpret_cast<const bf16x8*>(p); }

struct LnRegs { u32x4 v[2][7]; };
DI void ln_load(LnRegs& r, const u16* __restrict__ x, int m0, int tid) {
  const int sub = tid & 7;
#pragma unroll
  for (int pass = 0; pass < 2; ++pass) {
    const u16* xr = x + (size_t)(m0 + pass * 64 + (tid >> 3)) * DM;
#pragma unroll
    for (int c = 0; c < 7; ++c) r.v[pass][c] = *reinterpret_cast<const u32x4*>(xr + (c * 8 + sub) * 8);
  }
}
template <int SA>
DI void ln_store(const LnRegs& r, char* sA, float* sinv, int tid) {
  const int sub = tid & 7;
#pragma unroll
  for (int pass = 0; pass < 2; ++pass) {
    const int row = pass * 64 + (tid >> 3);
    float sum = 0.f;
#pragma unroll
    for (int c = 0; c < 7; ++c)
#pragma unroll
      for (int j = 0; j < 4; ++j)
        sum += __uint_as_float(r.v[pass][c][j] << 16) + __uint_as_float(r.v[pass][c][j] & 0xffff0000u);
    sum += __shfl_xor(sum, 1);
    sum += __shfl_xor(sum, 2);
    sum += __shfl_xor(sum, 4);
    const float mean = sum * (1.0f / DM);
    const float mean_b = bfr(mean);
    float sq = 0.f;
#pragma unroll
    for (int c = 0; c < 7; ++c) {
      u32x4 o;
#pragma unroll
      for (int j = 0; j < 4; ++j) {
        const float f0 = __uint_as_float(r.v[pass][c][j] << 16);
        const float f1 = __uint_as_float(r.v[pass][c][j] & 0xffff0000u);
        const float d0 = f0 - mean, d1 = f1 - mean;
        sq = fmaf(d0, d0, fmaf(d1, d1, sq));
        o[j] = pack2(f0 - mean_b, f1 - mean_b);
      }
      *reinterpret_cast<u32x4*>(sA + row * SA + (c * 8 + sub) * 16) = o;
    }
    sq += __shfl_xor(sq, 1);
    sq += __shfl_xor(sq, 2);
    sq += __shfl_xor(sq, 4);
    if (sub == 0) sinv[row] = 1.0f / bfr(sqrtf(bfr(bfr(sq * (1.0f / DM)) + 1e-5f)));
  }
}

#define QKV_SA 912
#define QKV_SB 192
__global__ __launch_bounds__(512) void k_ln_qkv(const u16* __restrict__ x, const u16* __restrict__ w,
                                                u16* __restrict__ QKV) {
  extern __shared__ __attribute__((aligned(16))) char smem[];
  char* sA = smem;
  char* sB = smem + 128 * QKV_SA;
  float* sinv = reinterpret_cast<float*>(smem + 128 * QKV_SA + 224 * QKV_SB);
  const int tid = threadIdx.x, lane = tid & 63, wv = tid >> 6;
  const int bm = blockIdx.x & 15, bn = blockIdx.x >> 4;
  const int m0 = bm * 128, n0 = bn * 96;
  const int wr = wv & 3, kp = wv >> 2;

  u32x4 br[11];
#pragma unroll
  for (int i = 0; i < 11; ++i) {
    const int c = tid + 512 * i, r = c / 12, c16 = c % 12;
    if (i < 10 || tid < 256)
      br[i] = *reinterpret_cast<const u32x4*>(w + (size_t)r * (3 * DM) + n0 + c16 * 8);
  }
  LnRegs lr;
  ln_load(lr, x, m0, tid);
  ln_store<QKV_SA>(lr, sA, sinv, tid);
#pragma unroll
  for (int i = 0; i < 6; ++i) {
    const int c = tid + 512 * i, r = c / 12, c16 = c % 12;
    if (c < 2688) *reinterpret_cast<u32x4*>(sB + r * QKV_SB + c16 * 16) = br[i];
  }
  __syncthreads();

  f32x16 acc[3];
#pragma unroll
  for (int t = 0; t < 3; ++t)
#pragma unroll
    for (int i = 0; i < 16; ++i) acc[t][i] = 0.f;

  const int l31 = lane & 31, h = lane >> 5, i16 = lane & 15, q4 = i16 >> 2, p4 = i16 & 3,
            blk = (lane >> 4) & 1;
  const char* aBase = sA + (wr * 32 + l31) * QKV_SA + 16 * h + kp * 32;
  const char* bBase = sB + (kp * 16 + 8 * h + q4) * QKV_SB + (16 * blk + 4 * p4) * 2;

#pragma unroll
  for (int half = 0; half < 2; ++half) {
    if (half == 1) {
      __syncthreads();
#pragma unroll
      for (int i = 5; i < 11; ++i) {
        const int c = tid + 512 * i, r = c / 12, c16 = c % 12;
        if (c >= 2688 && c < 5376) *reinterpret_cast<u32x4*>(sB + (r - 224) * QKV_SB + c16 * 16) = br[i];
      }
      __syncthreads();
    }
#pragma unroll
    for (int ks = 0; ks < 7; ++ks) {
      const bf16x8 xf = lds_b128(aBase + (half * 224 + ks * 32) * 2);
#pragma unroll
      for (int t = 0; t < 3; ++t) {
        const bf16x8 wf = tr_frag(bBase + ks * 32 * QKV_SB + t * 64, QKV_SB);
        acc[t] = MFMA(wf, xf, acc[t]);
      }
    }
  }

  __syncthreads();
  float* red = reinterpret_cast<float*>(sA);
#pragma unroll
  for (int t = 0; t < 3; ++t)
#pragma unroll
    for (int i = 0; i < 16; ++i) {
      const int r = t * 16 + i;
      const int owner = r < 24 ? 0 : 1;
      if (owner != kp) red[((wr * 2 + kp) * 24 + (r % 24)) * 64 + lane] = acc[t][i];
    }
  __syncthreads();
  {
    const int m = m0 + wr * 32 + l31;
    const float inv = sinv[wr * 32 + l31];
#pragma unroll
    for (int t = 0; t < 3; ++t) {
      const int ns = n0 + 32 * t;
      const int which = ns / DM, cb = ns % DM;
      const float sc = which == 0 ? inv * (0.18898223650461362f * 1.4426950408889634f) : inv;
#pragma unroll
      for (int g = 0; g < 4; ++g) {
        const int r0 = t * 16 + 4 * g;
        const int owner = r0 < 24 ? 0 : 1;
        if (owner == kp) {
          float v[4];
#pragma unroll
          for (int j = 0; j < 4; ++j)
            v[j] = (acc[t][4 * g + j] + red[((wr * 2 + (1 - kp)) * 24 + ((r0 + j) % 24)) * 64 + lane]) * sc;
          const int c = cb + 8 * g + 4 * h;
          const int head = c / DH, d = c % DH;
          u32x2 o;
          o[0] = pack2(v[0], v[1]);
          o[1] = pack2(v[2], v[3]);
          u16* p = QKV + ((size_t)(which * NH + head) * S_LEN + m) * 32 + d;
          *reinterpret_cast<u32x2*>(p) = o;
          if (d == 24) *reinterpret_cast<u32x2*>(p + 4) = (u32x2){which == 2 ? 0x3f80u : 0u, 0u};
        }
      }
    }
  }
}

#define AT_SK 80
#define AT_SV 64
#define AT_KB (128 * AT_SK)
#define AT_VB (128 * AT_SV)
#define AT_BUF (2 * AT_KB + 2 * AT_VB)
__global__ __launch_bounds__(512) void k_attn(const u16* __restrict__ QKV, u16* __restrict__ O) {
  extern __shared__ __attribute__((aligned(16))) char smem[];
  const int tid = threadIdx.x, lane = tid & 63, wv = tid >> 6;
  const int bid = blockIdx.x;
  const int head = (bid & 7) * 2 + ((bid >> 3) & 1), qt = bid >> 4;
  const int l31 = lane & 31, h = lane >> 5, i16 = lane & 15, q4 = i16 >> 2, p4 = i16 & 3,
            blk = (lane >> 4) & 1;
  const int qs = wv & 3, kh = wv >> 2;

  const u16* Qh = QKV + (size_t)head * S_LEN * 32;
  const u16* Kh = QKV + (size_t)(NH + head) * S_LEN * 32;
  const u16* Vh = QKV + (size_t)(2 * NH + head) * S_LEN * 32;

  const int qrow = qt * 128 + qs * 32 + l31;
  const u16* qp = Qh + (size_t)qrow * 32 + 8 * h;
  const bf16x8 qf0 = *reinterpret_cast<const bf16x8*>(qp);
  const bf16x8 qf1 = *reinterpret_cast<const bf16x8*>(qp + 16);

  u32x4 kr[2], vr[2];
  auto loadKV = [&](int it) {
#pragma unroll
    for (int i = 0; i < 2; ++i) {
      const size_t off = (size_t)(i * 1024 + it * 128) * 32 + tid * 8;
      kr[i] = *reinterpret_cast<const u32x4*>(Kh + off);
      vr[i] = *reinterpret_cast<const u32x4*>(Vh + off);
    }
  };
  auto storeKV = [&](int buf) {
    char* b = smem + buf * AT_BUF;
#pragma unroll
    for (int i = 0; i < 2; ++i) {
      *reinterpret_cast<u32x4*>(b + i * AT_KB + (tid >> 2) * AT_SK + (tid & 3) * 16) = kr[i];
      *reinterpret_cast<u32x4*>(b + 2 * AT_KB + i * AT_VB + tid * 16) = vr[i];
    }
  };
  loadKV(0);
  storeKV(0);
  __syncthreads();

  float m = 0.f;
  f32x16 negm, o;
#pragma unroll
  for (int i = 0; i < 16; ++i) { negm[i] = 0.f; o[i] = 0.f; }

#pragma unroll 1
  for (int it = 0; it < 8; ++it) {
    if (it < 7) loadKV(it + 1);
    const char* kb = smem + (it & 1) * AT_BUF + kh * AT_KB + l31 * AT_SK + 16 * h;
    const char* vb = smem + (it & 1) * AT_BUF + 2 * AT_KB + kh * AT_VB + (4 * h + q4) * AT_SV +
                     (16 * blk + 4 * p4) * 2;
    f32x16 s[4];
#pragma unroll
    for (int t = 0; t < 4; ++t) {
      s[t] = MFMA(lds_b128(kb + t * 32 * AT_SK), qf0, negm);
      s[t] = MFMA(lds_b128(kb + t * 32 * AT_SK + 32), qf1, s[t]);
    }
    float mx = s[0][0];
#pragma unroll
    for (int t = 0; t < 4; ++t)
#pragma unroll
      for (int i = 0; i < 16; ++i) mx = fmaxf(mx, s[t][i]);
    mx = fmaxf(mx, __shfl_xor(mx, 32));
    const float delta = it == 0 ? mx : fmaxf(mx, 0.f);
    if (__any(delta != 0.f)) {
      const float alpha = __builtin_amdgcn_exp2f(-delta);
      m += delta;
#pragma unroll
      for (int i = 0; i < 16; ++i) { negm[i] = -m; o[i] *= alpha; }
#pragma unroll
      for (int t = 0; t < 4; ++t)
#pragma unroll
        for (int i = 0; i < 16; ++i) s[t][i] -= delta;
    }
#pragma unroll
    for (int t = 0; t < 4; ++t) {
#pragma unroll
      for (int sp = 0; sp < 2; ++sp) {
        u32x4 pk;
#pragma unroll
        for (int j = 0; j < 4; ++j)
          pk[j] = pack2(__builtin_amdgcn_exp2f(s[t][8 * sp + 2 * j]), __builtin_amdgcn_exp2f(s[t][8 * sp + 2 * j + 1]));
        const bf16x8 pf = __builtin_bit_cast(bf16x8, pk);
        const s16x4 lo = tr_read(vb + (t * 32 + sp * 16) * AT_SV);
        const s16x4 hi = tr_read(vb + (t * 32 + sp * 16 + 8) * AT_SV);
        const bf16x8 vf = __builtin_shufflevector(lo, hi, 0, 1, 2, 3, 4, 5, 6, 7);
        o = MFMA(vf, pf, o);
      }
    }
    if (it < 7) storeKV((it + 1) & 1);
    __syncthreads();
  }

  float* red = reinterpret_cast<float*>(smem);
  if (kh == 1) {
    red[(qs * 17 + 16) * 64 + lane] = m;
#pragma unroll
    for (int i = 0; i < 16; ++i) red[(qs * 17 + i) * 64 + lane] = o[i];
  }
  __syncthreads();
  if (kh == 0) {
    const float m2 = red[(qs * 17 + 16) * 64 + lane];
    const float mn = fmaxf(m, m2);
    const float a1 = __builtin_amdgcn_exp2f(m - mn);
    const float a2 = __builtin_amdgcn_exp2f(m2 - mn);
#pragma unroll
    for (int i = 0; i < 16; ++i) o[i] = o[i] * a1 + red[(qs * 17 + i) * 64 + lane] * a2;
    const float l = __shfl(o[12], l31 + 32);
    const float inv = 1.0f / l;
    u16* op = O + (size_t)qrow * DM + head * DH;
#pragma unroll
    for (int g = 0; g < 4; ++g) {
      const int d = 8 * g + 4 * h;
      if (d < DH) {
        u32x2 v;
        v[0] = pack2(o[4 * g] * inv, o[4 * g + 1] * inv);
        v[1] = pack2(o[4 * g + 2] * inv, o[4 * g + 3] * inv);
        *reinterpret_cast<u32x2*>(op + d) = v;
      }
    }
  }
}

#define GR_SA 464
#define GR_SB 192
#define GR_BUF (64 * GR_SA + 224 * GR_SB)
template <int KTOT>
__global__ __launch_bounds__(512) void k_gemm_res(const u16* __restrict__ A, const u16* __restrict__ W,
                                                  const u16* __restrict__ res, u16* __restrict__ out) {
  extern __shared__ __attribute__((aligned(16))) char smem[];
  const int tid = threadIdx.x, lane = tid & 63, wv = tid >> 6;
  const int bm = 2 * ((blockIdx.x & 7) + 8 * ((blockIdx.x >> 4) & 1)) + ((blockIdx.x >> 3) & 1);
  const int bn = blockIdx.x >> 5;
  const int m0 = bm * 64, n0 = bn * 64;
  constexpr int NC = KTOT / 224;
  constexpr int PD = NC < 3 ? NC : 3;

  const int wm = wv & 1, wn = (wv >> 1) & 1, kp = wv >> 2;
  const int l31 = lane & 31, h = lane >> 5, i16 = lane & 15, q4 = i16 >> 2, p4 = i16 & 3,
            blk = (lane >> 4) & 1;

  u32x4 ar[PD][4], br[PD][4];
  auto load = [&](int kc, int slot) {
#pragma unroll
    for (int i = 0; i < 4; ++i) {
      const int c = tid + 512 * i;
      if (i < 3 || tid < 256) {
        const int r = c / 28, c16 = c % 28;
        ar[slot][i] = *reinterpret_cast<const u32x4*>(A + (size_t)(m0 + r) * KTOT + kc * 224 + c16 * 8);
        const int rb = c >> 3, cb = c & 7;
        br[slot][i] = *reinterpret_cast<const u32x4*>(W + (size_t)(kc * 224 + rb) * DM + n0 + cb * 8);
      }
    }
  };
  auto store = [&](int slot, int buf) {
    char* b = smem + buf * GR_BUF;
#pragma unroll
    for (int i = 0; i < 4; ++i) {
      const int c = tid + 512 * i;
      if (i < 3 || tid < 256) {
        const int r = c / 28, c16 = c % 28;
        *reinterpret_cast<u32x4*>(b + r * GR_SA + c16 * 16) = ar[slot][i];
        const int rb = c >> 3, cb = c & 7;
        *reinterpret_cast<u32x4*>(b + 64 * GR_SA + rb * GR_SB + cb * 16) = br[slot][i];
      }
    }
  };
#pragma unroll
  for (int i = 0; i < PD; ++i) load(i, i);
  const int m = m0 + wm * 32 + l31;
  u32x2 rr[4];
#pragma unroll
  for (int g = 0; g < 4; ++g)
    rr[g] = *reinterpret_cast<const u32x2*>(res + (size_t)m * DM + n0 + wn * 32 + 8 * g + 4 * h);
  store(0, 0);
  if (PD < NC) load(PD, 0);
  __syncthreads();

  const int aOff = (wm * 32 + l31) * GR_SA + 16 * h + kp * 32;
  const int bOff = 64 * GR_SA + (kp * 16 + 8 * h + q4) * GR_SB + (wn * 32 + 16 * blk + 4 * p4) * 2;

  f32x16 acc, acc2;
#pragma unroll
  for (int i = 0; i < 16; ++i) { acc[i] = 0.f; acc2[i] = 0.f; }

#pragma unroll
  for (int kc = 0; kc < NC; ++kc) {
    const char* b = smem + (kc & 1) * GR_BUF;
#pragma unroll
    for (int ks = 0; ks < 7; ++ks) {
      const bf16x8 xf = lds_b128(b + aOff + ks * 64);
      const bf16x8 wf = tr_frag(b + bOff + ks * 32 * GR_SB, GR_SB);
      if (ks & 1) acc2 = MFMA(wf, xf, acc2); else acc = MFMA(wf, xf, acc);
    }
    if (kc + 1 < NC) {
      store((kc + 1) % PD, (kc + 1) & 1);
      if (kc + 1 + PD < NC) load(kc + 1 + PD, (kc + 1) % PD);
      __syncthreads();
    }
  }
#pragma unroll
  for (int i = 0; i < 16; ++i) acc[i] += acc2[i];

  __syncthreads();
  float* red = reinterpret_cast<float*>(smem);
  if (kp == 1) {
#pragma unroll
    for (int i = 0; i < 16; ++i) red[((wv & 3) * 16 + i) * 64 + lane] = acc[i];
  }
  __syncthreads();
  if (kp == 0) {
#pragma unroll
    for (int i = 0; i < 16; ++i) acc[i] += red[((wv & 3) * 16 + i) * 64 + lane];
#pragma unroll
    for (int g = 0; g < 4; ++g) {
      const size_t off = (size_t)m * DM + n0 + wn * 32 + 8 * g + 4 * h;
      u32x2 o;
      o[0] = pack2(__uint_as_float(rr[g][0] << 16) + bfr(acc[4 * g]),
                   __uint_as_float(rr[g][0] & 0xffff0000u) + bfr(acc[4 * g + 1]));
      o[1] = pack2(__uint_as_float(rr[g][1] << 16) + bfr(acc[4 * g + 2]),
                   __uint_as_float(rr[g][1] & 0xffff0000u) + bfr(acc[4 * g + 3]));
      *reinterpret_cast<u32x2*>(out + off) = o;
    }
  }
}

#define UP_SA 912
#define UP_SB 320
__global__ __launch_bounds__(512) void k_ln_up(const u16* __restrict__ x, const u16* __restrict__ w1,
                                               const u16* __restrict__ w3, u16* __restrict__ Hb) {
  extern __shared__ __attribute__((aligned(16))) char smem[];
  char* sA = smem;
  char* sB = smem + 128 * UP_SA;
  float* sinv = reinterpret_cast<float*>(smem + 128 * UP_SA + 2 * 64 * UP_SB);
  const int tid = threadIdx.x, lane = tid & 63, wv = tid >> 6;
  const int bm = blockIdx.x & 15, bn = blockIdx.x >> 4;
  const int m0 = bm * 128, n0 = bn * 128;

  u32x4 br[2][4];
  const int brow = tid >> 4, bc = tid & 15;
  auto loadB = [&](int kt, int slot) {
#pragma unroll
    for (int i = 0; i < 2; ++i) {
      const size_t off = (size_t)(kt * 64 + brow + 32 * i) * FF + n0 + bc * 8;
      br[slot][i] = *reinterpret_cast<const u32x4*>(w1 + off);
      br[slot][2 + i] = *reinterpret_cast<const u32x4*>(w3 + off);
    }
  };
  auto storeB = [&](int slot) {
#pragma unroll
    for (int i = 0; i < 2; ++i) {
      *reinterpret_cast<u32x4*>(sB + (brow + 32 * i) * UP_SB + bc * 16) = br[slot][i];
      *reinterpret_cast<u32x4*>(sB + 64 * UP_SB + (brow + 32 * i) * UP_SB + bc * 16) = br[slot][2 + i];
    }
  };
  loadB(0, 0);
  loadB(1, 1);
  LnRegs lr;
  ln_load(lr, x, m0, tid);
  ln_store<UP_SA>(lr, sA, sinv, tid);
  storeB(0);
  __syncthreads();

  const int wm = wv & 1, wn = wv >> 1;
  const int l31 = lane & 31, h = lane >> 5, i16 = lane & 15, q4 = i16 >> 2, p4 = i16 & 3,
            blk = (lane >> 4) & 1;
  const char* aBase = sA + (wm * 64 + l31) * UP_SA + 16 * h;
  const char* bBase = sB + (8 * h + q4) * UP_SB + (wn * 32 + 16 * blk + 4 * p4) * 2;

  f32x16 acc[2][2];
#pragma unroll
  for (int a = 0; a < 2; ++a)
#pragma unroll
    for (int c = 0; c < 2; ++c)
#pragma unroll
      for (int i = 0; i < 16; ++i) acc[a][c][i] = 0.f;

#pragma unroll
  for (int kt = 0; kt < 7; ++kt) {
#pragma unroll
    for (int ks = 0; ks < 4; ++ks) {
      bf16x8 xf[2];
#pragma unroll
      for (int mt = 0; mt < 2; ++mt) xf[mt] = lds_b128(aBase + mt * 32 * UP_SA + (kt * 64 + ks * 16) * 2);
#pragma unroll
      for (int a = 0; a < 2; ++a) {
        const bf16x8 wf = tr_frag(bBase + a * 64 * UP_SB + ks * 16 * UP_SB, UP_SB);
#pragma unroll
        for (int mt = 0; mt < 2; ++mt) acc[a][mt] = MFMA(wf, xf[mt], acc[a][mt]);
      }
    }
    if (kt < 6) {
      __syncthreads();
      storeB((kt + 1) & 1);
      if (kt < 5) loadB(kt + 2, kt & 1);
      __syncthreads();
    }
  }

#pragma unroll
  for (int mt = 0; mt < 2; ++mt) {
    const int m = m0 + wm * 64 + mt * 32 + l31;
    const float inv = sinv[wm * 64 + mt * 32 + l31];
#pragma unroll
    for (int g = 0; g < 4; ++g) {
      float hv[4];
#pragma unroll
      for (int j = 0; j < 4; ++j) {
        const float gt = bfr(acc[0][mt][4 * g + j] * inv);
        const float up = bfr(acc[1][mt][4 * g + j] * inv);
        const float sl = bfr(gt / (1.0f + __expf(-gt)));
        hv[j] = sl * up;
      }
      u32x2 o;
      o[0] = pack2(hv[0], hv[1]);
      o[1] = pack2(hv[2], hv[3]);
      *reinterpret_cast<u32x2*>(Hb + (size_t)m * FF + n0 + wn * 32 + 8 * g + 4 * h) = o;
    }
  }
}

extern "C" void kernel_launch(void* const* d_in, const int* in_sizes, int n_in, void* d_out, int out_size,
                              void* d_ws, size_t ws_size, hipStream_t stream) {
  const u16* x = (const u16*)d_in[0];
  const u16* wqkv = (const u16*)d_in[1];
  const u16* wo = (const u16*)d_in[2];
  const u16* w1 = (const u16*)d_in[4];
  const u16* w3 = (const u16*)d_in[5];
  const u16* w2 = (const u16*)d_in[6];
  u16* out = (u16*)d_out;

  char* ws = (char*)d_ws;
  u16* QKV = (u16*)(ws);
  u16* Ob = (u16*)(ws + (6u << 20));
  u16* x1 = (u16*)(ws + (8u << 20));
  u16* x2 = (u16*)(ws + (10u << 20));
  u16* x3 = (u16*)(ws + (12u << 20));
  u16* Hb = (u16*)(ws + (16u << 20));

  const int ldsQKV = 128 * QKV_SA + 224 * QKV_SB + 512;
  const int ldsAT = 2 * AT_BUF;
  const int ldsGR = 2 * GR_BUF;
  const int ldsUP = 128 * UP_SA + 2 * 64 * UP_SB + 512;
  (void)hipFuncSetAttribute((const void*)k_ln_qkv, hipFuncAttributeMaxDynamicSharedMemorySize, ldsQKV);
  (void)hipFuncSetAttribute((const void*)k_attn, hipFuncAttributeMaxDynamicSharedMemorySize, ldsAT);
  (void)hipFuncSetAttribute((const void*)k_gemm_res<448>, hipFuncAttributeMaxDynamicSharedMemorySize, ldsGR);
  (void)hipFuncSetAttribute((const void*)k_gemm_res<1792>, hipFuncAttributeMaxDynamicSharedMemorySize, ldsGR);
  (void)hipFuncSetAttribute((const void*)k_ln_up, hipFuncAttributeMaxDynamicSharedMemorySize, ldsUP);

  const u16* xin = x;
  for (int layer = 0; layer < 2; ++layer) {
    u16* xa = layer == 0 ? x1 : x3;
    u16* xb = layer == 0 ? x2 : out;
    k_ln_qkv<<<224, 512, ldsQKV, stream>>>(xin, wqkv, QKV);
    k_attn<<<256, 512, ldsAT, stream>>>(QKV, Ob);
    k_gemm_res<448><<<224, 512, ldsGR, stream>>>(Ob, wo, xin, xa);
    k_ln_up<<<224, 512, ldsUP, stream>>>(xa, w1, w3, Hb);
    k_gemm_res<1792><<<224, 512, ldsGR, stream>>>(Hb, w2, xa, xb);
    xin = xb;
  }
}
```

```cpp
#include <hip/hip_runtime.h>
#include <hip/hip_bf16.h>
#include <stdint.h>


#define S_LEN 2048
#define DM 448
#define NH 16
#define DH 28
#define FF 1792

typedef __attribute__((ext_vector_type(8))) short bf16x8;
typedef __attribute__((ext_vector_type(4))) short s16x4;
typedef __attribute__((ext_vector_type(16))) float f32x16;
typedef __attribute__((ext_vector_type(4))) unsigned u32x4;
typedef __attribute__((ext_vector_type(2))) unsigned u32x2;
typedef unsigned short u16;

#define DI __device__ __forceinline__
#define MFMA(a, b, c) __builtin_amdgcn_mfma_f32_32x32x16_bf16((a), (b), (c), 0, 0, 0)

DI float bf2f(u16 v) { return __uint_as_float(((unsigned)v) << 16); }
DI u16 f2bf(float f) {
  __hip_bfloat16 b = __float2bfloat16(f);
  return *reinterpret_cast<u16*>(&b);
}
DI float bfr(float f) { return bf2f(f2bf(f)); }
typedef __attribute__((ext_vector_type(2))) float f32x2;
typedef __attribute__((ext_vector_type(2))) __bf16 bf16x2;
DI unsigned pack2(float lo, float hi) {
  const f32x2 v = {lo, hi};
  return __builtin_bit_cast(unsigned, __builtin_convertvector(v, bf16x2));
}

DI s16x4 tr_read(const char* p) {
  return __builtin_amdgcn_ds_read_tr16_b64_v4i16(
      (__attribute__((address_space(3))) s16x4*)(p));
}
DI bf16x8 tr_frag(const char* p, int stride) {
  s16x4 lo = tr_read(p);
  s16x4 hi = tr_read(p + 4 * stride);
  return __builtin_shufflevector(lo, hi, 0, 1, 2, 3, 4, 5, 6, 7);
}
DI bf16x8 lds_b128(const char* p) { return *reinterpret_cast<const bf16x8*>(p); }

struct LnRegs { u32x4 v[2][7]; };
DI void ln_load(LnRegs& r, const u16* __restrict__ x, int m0, int tid) {
  const int sub = tid & 7;
#pragma unroll
  for (int pass = 0; pass < 2; ++pass) {
    const u16* xr = x + (size_t)(m0 + pass * 64 + (tid >> 3)) * DM;
#pragma unroll
    for (int c = 0; c < 7; ++c) r.v[pass][c] = *reinterpret_cast<const u32x4*>(xr + (c * 8 + sub) * 8);
  }
}
template <int SA>
DI void ln_store(const LnRegs& r, char* sA, float2* st, int tid) {
  const int sub = tid & 7;
#pragma unroll
  for (int pass = 0; pass < 2; ++pass) {
    const int row = pass * 64 + (tid >> 3);
    float sum = 0.f, sq = 0.f;
#pragma unroll
    for (int c = 0; c < 7; ++c) {
      *reinterpret_cast<u32x4*>(sA + row * SA + (c * 8 + sub) * 16) = r.v[pass][c];
#pragma unroll
      for (int j = 0; j < 4; ++j) {
        const float f0 = __uint_as_float(r.v[pass][c][j] << 16);
        const float f1 = __uint_as_float(r.v[pass][c][j] & 0xffff0000u);
        sum += f0 + f1;
        sq = fmaf(f0, f0, fmaf(f1, f1, sq));
      }
    }
    sum += __shfl_xor(sum, 1);
    sq += __shfl_xor(sq, 1);
    sum += __shfl_xor(sum, 2);
    sq += __shfl_xor(sq, 2);
    sum += __shfl_xor(sum, 4);
    sq += __shfl_xor(sq, 4);
    const float mean = sum * (1.0f / DM);
    const float var = fmaxf(fmaf(-mean, mean, sq * (1.0f / DM)), 0.f);
    if (sub == 0) st[row] = make_float2(mean, 1.0f / sqrtf(var + 1e-5f));
  }
}

#define QKV_SA 912
#define QKV_SB 192
__global__ __launch_bounds__(512) void k_ln_qkv(const u16* __restrict__ x, const u16* __restrict__ w,
                                                u16* __restrict__ QKV) {
  extern __shared__ __attribute__((aligned(16))) char smem[];
  char* sA = smem;
  char* sB = smem + 128 * QKV_SA;
  float2* st = reinterpret_cast<float2*>(smem + 128 * QKV_SA + 224 * QKV_SB);
  const int tid = threadIdx.x, lane = tid & 63, wv = tid >> 6;
  const int bm = blockIdx.x & 15, bn = blockIdx.x >> 4;
  const int m0 = bm * 128, n0 = bn * 96;
  const int wr = wv & 3, kp = wv >> 2;

  u32x4 br[11];
#pragma unroll
  for (int i = 0; i < 11; ++i) {
    const int c = tid + 512 * i, r = c / 12, c16 = c % 12;
    if (i < 10 || tid < 256)
      br[i] = *reinterpret_cast<const u32x4*>(w + (size_t)r * (3 * DM) + n0 + c16 * 8);
  }
  LnRegs lr;
  ln_load(lr, x, m0, tid);
  ln_store<QKV_SA>(lr, sA, st, tid);
#pragma unroll
  for (int i = 0; i < 6; ++i) {
    const int c = tid + 512 * i, r = c / 12, c16 = c % 12;
    if (c < 2688) *reinterpret_cast<u32x4*>(sB + r * QKV_SB + c16 * 16) = br[i];
  }
  __syncthreads();

  f32x16 acc[3], cs[3];
#pragma unroll
  for (int t = 0; t < 3; ++t)
#pragma unroll
    for (int i = 0; i < 16; ++i) { acc[t][i] = 0.f; cs[t][i] = 0.f; }
  const bf16x8 ones = {0x3f80, 0x3f80, 0x3f80, 0x3f80, 0x3f80, 0x3f80, 0x3f80, 0x3f80};

  const int l31 = lane & 31, h = lane >> 5, i16 = lane & 15, q4 = i16 >> 2, p4 = i16 & 3,
            blk = (lane >> 4) & 1;
  const char* aBase = sA + (wr * 32 + l31) * QKV_SA + 16 * h + kp * 32;
  const char* bBase = sB + (kp * 16 + 8 * h + q4) * QKV_SB + (16 * blk + 4 * p4) * 2;

#pragma unroll
  for (int half = 0; half < 2; ++half) {
    if (half == 1) {
      __syncthreads();
#pragma unroll
      for (int i = 5; i < 11; ++i) {
        const int c = tid + 512 * i, r = c / 12, c16 = c % 12;
        if (c >= 2688 && c < 5376) *reinterpret_cast<u32x4*>(sB + (r - 224) * QKV_SB + c16 * 16) = br[i];
      }
      __syncthreads();
    }
#pragma unroll
    for (int ks = 0; ks < 7; ++ks) {
      const bf16x8 xf = lds_b128(aBase + (half * 224 + ks * 32) * 2);
#pragma unroll
      for (int t = 0; t < 3; ++t) {
        const bf16x8 wf = tr_frag(bBase + ks * 32 * QKV_SB + t * 64, QKV_SB);
        acc[t] = MFMA(wf, xf, acc[t]);
        cs[t] = MFMA(wf, ones, cs[t]);
      }
    }
  }

  const float2 mst = st[wr * 32 + l31];
#pragma unroll
  for (int t = 0; t < 3; ++t)
#pragma unroll
    for (int i = 0; i < 16; ++i) acc[t][i] = fmaf(-mst.x, cs[t][i], acc[t][i]);
  __syncthreads();
  float* red = reinterpret_cast<float*>(sA);
#pragma unroll
  for (int t = 0; t < 3; ++t)
#pragma unroll
    for (int i = 0; i < 16; ++i) {
      const int r = t * 16 + i;
      const int owner = r < 24 ? 0 : 1;
      if (owner != kp) red[((wr * 2 + kp) * 24 + (r % 24)) * 64 + lane] = acc[t][i];
    }
  __syncthreads();
  {
    const int m = m0 + wr * 32 + l31;
    const float inv = mst.y;
#pragma unroll
    for (int t = 0; t < 3; ++t) {
      const int ns = n0 + 32 * t;
      const int which = ns / DM, cb = ns % DM;
      const float sc = which == 0 ? inv * (0.18898223650461362f * 1.4426950408889634f) : inv;
#pragma unroll
      for (int g = 0; g < 4; ++g) {
        const int r0 = t * 16 + 4 * g;
        const int owner = r0 < 24 ? 0 : 1;
        if (owner == kp) {
          float v[4];
#pragma unroll
          for (int j = 0; j < 4; ++j)
            v[j] = (acc[t][4 * g + j] + red[((wr * 2 + (1 - kp)) * 24 + ((r0 + j) % 24)) * 64 + lane]) * sc;
          const int c = cb + 8 * g + 4 * h;
          const int head = c / DH, d = c % DH;
          u32x2 o;
          o[0] = pack2(v[0], v[1]);
          o[1] = pack2(v[2], v[3]);
          u16* p = QKV + ((size_t)(which * NH + head) * S_LEN + m) * 32 + d;
          *reinterpret_cast<u32x2*>(p) = o;
          if (d == 24) *reinterpret_cast<u32x2*>(p + 4) = (u32x2){which == 2 ? 0x3f80u : 0u, 0u};
        }
      }
    }
  }
}

#define AT_SK 80
#define AT_SV 64
#define AT_KB (128 * AT_SK)
#define AT_VB (128 * AT_SV)
#define AT_BUF (2 * AT_KB + 2 * AT_VB)
__global__ __launch_bounds__(512) void k_attn(const u16* __restrict__ QKV, u16* __restrict__ O) {
  extern __shared__ __attribute__((aligned(16))) char smem[];
  const int tid = threadIdx.x, lane = tid & 63, wv = tid >> 6;
  const int bid = blockIdx.x;
  const int head = (bid & 7) * 2 + ((bid >> 3) & 1), qt = bid >> 4;
  const int l31 = lane & 31, h = lane >> 5, i16 = lane & 15, q4 = i16 >> 2, p4 = i16 & 3,
            blk = (lane >> 4) & 1;
  const int qs = wv & 3, kh = wv >> 2;

  const u16* Qh = QKV + (size_t)head * S_LEN * 32;
  const u16* Kh = QKV + (size_t)(NH + head) * S_LEN * 32;
  const u16* Vh = QKV + (size_t)(2 * NH + head) * S_LEN * 32;

  const int qrow = qt * 128 + qs * 32 + l31;
  const u16* qp = Qh + (size_t)qrow * 32 + 8 * h;
  const bf16x8 qf0 = *reinterpret_cast<const bf16x8*>(qp);
  const bf16x8 qf1 = *reinterpret_cast<const bf16x8*>(qp + 16);

  u32x4 kr[2], vr[2];
  auto loadKV = [&](int it) {
#pragma unroll
    for (int i = 0; i < 2; ++i) {
      const size_t off = (size_t)(i * 1024 + it * 128) * 32 + tid * 8;
      kr[i] = *reinterpret_cast<const u32x4*>(Kh + off);
      vr[i] = *reinterpret_cast<const u32x4*>(Vh + off);
    }
  };
  auto storeKV = [&](int buf) {
    char* b = smem + buf * AT_BUF;
#pragma unroll
    for (int i = 0; i < 2; ++i) {
      *reinterpret_cast<u32x4*>(b + i * AT_KB + (tid >> 2) * AT_SK + (tid & 3) * 16) = kr[i];
      *reinterpret_cast<u32x4*>(b + 2 * AT_KB + i * AT_VB + tid * 16) = vr[i];
    }
  };
  loadKV(0);
  storeKV(0);
  __syncthreads();

  float m = 0.f;
  f32x16 negm, o;
#pragma unroll
  for (int i = 0; i < 16; ++i) { negm[i] = 0.f; o[i] = 0.f; }

#pragma unroll 1
  for (int it = 0; it < 8; ++it) {
    if (it < 7) loadKV(it + 1);
    const char* kb = smem + (it & 1) * AT_BUF + kh * AT_KB + l31 * AT_SK + 16 * h;
    const char* vb = smem + (it & 1) * AT_BUF + 2 * AT_KB + kh * AT_VB + (4 * h + q4) * AT_SV +
                     (16 * blk + 4 * p4) * 2;
    f32x16 s[4];
#pragma unroll
    for (int t = 0; t < 4; ++t) {
      s[t] = MFMA(lds_b128(kb + t * 32 * AT_SK), qf0, negm);
      s[t] = MFMA(lds_b128(kb + t * 32 * AT_SK + 32), qf1, s[t]);
    }
    float mx = s[0][0];
#pragma unroll
    for (int t = 0; t < 4; ++t)
#pragma unroll
      for (int i = 0; i < 16; ++i) mx = fmaxf(mx, s[t][i]);
    mx = fmaxf(mx, __shfl_xor(mx, 32));
    const float delta = it == 0 ? mx : fmaxf(mx, 0.f);
    if (__any(delta != 0.f)) {
      const float alpha = __builtin_amdgcn_exp2f(-delta);
      m += delta;
#pragma unroll
      for (int i = 0; i < 16; ++i) { negm[i] = -m; o[i] *= alpha; }
#pragma unroll
      for (int t = 0; t < 4; ++t)
#pragma unroll
        for (int i = 0; i < 16; ++i) s[t][i] -= delta;
    }
#pragma unroll
    for (int t = 0; t < 4; ++t) {
#pragma unroll
      for (int sp = 0; sp < 2; ++sp) {
        u32x4 pk;
#pragma unroll
        for (int j = 0; j < 4; ++j)
          pk[j] = pack2(__builtin_amdgcn_exp2f(s[t][8 * sp + 2 * j]), __builtin_amdgcn_exp2f(s[t][8 * sp + 2 * j + 1]));
        const bf16x8 pf = __builtin_bit_cast(bf16x8, pk);
        const s16x4 lo = tr_read(vb + (t * 32 + sp * 16) * AT_SV);
        const s16x4 hi = tr_read(vb + (t * 32 + sp * 16 + 8) * AT_SV);
        const bf16x8 vf = __builtin_shufflevector(lo, hi, 0, 1, 2, 3, 4, 5, 6, 7);
        o = MFMA(vf, pf, o);
      }
    }
    if (it < 7) storeKV((it + 1) & 1);
    __syncthreads();
  }

  float* red = reinterpret_cast<float*>(smem);
  if (kh == 1) {
    red[(qs * 17 + 16) * 64 + lane] = m;
#pragma unroll
    for (int i = 0; i < 16; ++i) red[(qs * 17 + i) * 64 + lane] = o[i];
  }
  __syncthreads();
  if (kh == 0) {
    const float m2 = red[(qs * 17 + 16) * 64 + lane];
    const float mn = fmaxf(m, m2);
    const float a1 = __builtin_amdgcn_exp2f(m - mn);
    const float a2 = __builtin_amdgcn_exp2f(m2 - mn);
#pragma unroll
    for (int i = 0; i < 16; ++i) o[i] = o[i] * a1 + red[(qs * 17 + i) * 64 + lane] * a2;
    const float l = __shfl(o[12], l31 + 32);
    const float inv = 1.0f / l;
    u16* op = O + (size_t)qrow * DM + head * DH;
#pragma unroll
    for (int g = 0; g < 4; ++g) {
      const int d = 8 * g + 4 * h;
      if (d < DH) {
        u32x2 v;
        v[0] = pack2(o[4 * g] * inv, o[4 * g + 1] * inv);
        v[1] = pack2(o[4 * g + 2] * inv, o[4 * g + 3] * inv);
        *reinterpret_cast<u32x2*>(op + d) = v;
      }
    }
  }
}

#define GR_SA 464
#define GR_SB 192
#define GR_BUF (64 * GR_SA + 224 * GR_SB)
template <int KTOT>
__global__ __launch_bounds__(512) void k_gemm_res(const u16* __restrict__ A, const u16* __restrict__ W,
                                                  const u16* __restrict__ res, u16* __restrict__ out) {
  extern __shared__ __attribute__((aligned(16))) char smem[];
  const int tid = threadIdx.x, lane = tid & 63, wv = tid >> 6;
  const int bm = 2 * ((blockIdx.x & 7) + 8 * ((blockIdx.x >> 4) & 1)) + ((blockIdx.x >> 3) & 1);
  const int bn = blockIdx.x >> 5;
  const int m0 = bm * 64, n0 = bn * 64;
  constexpr int NC = KTOT / 224;
  constexpr int PD = NC < 3 ? NC : 3;

  const int wm = wv & 1, wn = (wv >> 1) & 1, kp = wv >> 2;
  const int l31 = lane & 31, h = lane >> 5, i16 = lane & 15, q4 = i16 >> 2, p4 = i16 & 3,
            blk = (lane >> 4) & 1;

  u32x4 ar[PD][4], br[PD][4];
  auto load = [&](int kc, int slot) {
#pragma unroll
    for (int i = 0; i < 4; ++i) {
      const int c = tid + 512 * i;
      if (i < 3 || tid < 256) {
        const int r = c / 28, c16 = c % 28;
        ar[slot][i] = *reinterpret_cast<const u32x4*>(A + (size_t)(m0 + r) * KTOT + kc * 224 + c16 * 8);
        const int rb = c >> 3, cb = c & 7;
        br[slot][i] = *reinterpret_cast<const u32x4*>(W + (size_t)(kc * 224 + rb) * DM + n0 + cb * 8);
      }
    }
  };
  auto store = [&](int slot, int buf) {
    char* b = smem + buf * GR_BUF;
#pragma unroll
    for (int i = 0; i < 4; ++i) {
      const int c = tid + 512 * i;
      if (i < 3 || tid < 256) {
        const int r = c / 28, c16 = c % 28;
        *reinterpret_cast<u32x4*>(b + r * GR_SA + c16 * 16) = ar[slot][i];
        const int rb = c >> 3, cb = c & 7;
        *reinterpret_cast<u32x4*>(b + 64 * GR_SA + rb * GR_SB + cb * 16) = br[slot][i];
      }
    }
  };
#pragma unroll
  for (int i = 0; i < PD; ++i) load(i, i);
  const int m = m0 + wm * 32 + l31;
  u32x2 rr[4];
#pragma unroll
  for (int g = 0; g < 4; ++g)
    rr[g] = *reinterpret_cast<const u32x2*>(res + (size_t)m * DM + n0 + wn * 32 + 8 * g + 4 * h);
  store(0, 0);
  if (PD < NC) load(PD, 0);
  __syncthreads();

  const int aOff = (wm * 32 + l31) * GR_SA + 16 * h + kp * 32;
  const int bOff = 64 * GR_SA + (kp * 16 + 8 * h + q4) * GR_SB + (wn * 32 + 16 * blk + 4 * p4) * 2;

  f32x16 acc, acc2;
#pragma unroll
  for (int i = 0; i < 16; ++i) { acc[i] = 0.f; acc2[i] = 0.f; }

#pragma unroll
  for (int kc = 0; kc < NC; ++kc) {
    const char* b = smem + (kc & 1) * GR_BUF;
#pragma unroll
    for (int ks = 0; ks < 7; ++ks) {
      const bf16x8 xf = lds_b128(b + aOff + ks * 64);
      const bf16x8 wf = tr_frag(b + bOff + ks * 32 * GR_SB, GR_SB);
      if (ks & 1) acc2 = MFMA(wf, xf, acc2); else acc = MFMA(wf, xf, acc);
    }
    if (kc + 1 < NC) {
      store((kc + 1) % PD, (kc + 1) & 1);
      if (kc + 1 + PD < NC) load(kc + 1 + PD, (kc + 1) % PD);
      __syncthreads();
    }
  }
#pragma unroll
  for (int i = 0; i < 16; ++i) acc[i] += acc2[i];

  __syncthreads();
  float* red = reinterpret_cast<float*>(smem);
  if (kp == 1) {
#pragma unroll
    for (int i = 0; i < 16; ++i) red[((wv & 3) * 16 + i) * 64 + lane] = acc[i];
  }
  __syncthreads();
  if (kp == 0) {
#pragma unroll
    for (int i = 0; i < 16; ++i) acc[i] += red[((wv & 3) * 16 + i) * 64 + lane];
#pragma unroll
    for (int g = 0; g < 4; ++g) {
      const size_t off = (size_t)m * DM + n0 + wn * 32 + 8 * g + 4 * h;
      u32x2 o;
      o[0] = pack2(__uint_as_float(rr[g][0] << 16) + bfr(acc[4 * g]),
                   __uint_as_float(rr[g][0] & 0xffff0000u) + bfr(acc[4 * g + 1]));
      o[1] = pack2(__uint_as_float(rr[g][1] << 16) + bfr(acc[4 * g + 2]),
                   __uint_as_float(rr[g][1] & 0xffff0000u) + bfr(acc[4 * g + 3]));
      *reinterpret_cast<u32x2*>(out + off) = o;
    }
  }
}

#define UP_SA 912
#define UP_SB 320
__global__ __launch_bounds__(512) void k_ln_up(const u16* __restrict__ x, const u16* __restrict__ w1,
                                               const u16* __restrict__ w3, u16* __restrict__ Hb) {
  extern __shared__ __attribute__((aligned(16))) char smem[];
  char* sA = smem;
  char* sB = smem + 128 * UP_SA;
  float2* st = reinterpret_cast<float2*>(smem + 128 * UP_SA + 2 * 64 * UP_SB);
  const int tid = threadIdx.x, lane = tid & 63, wv = tid >> 6;
  const int bm = blockIdx.x & 15, bn = blockIdx.x >> 4;
  const int m0 = bm * 128, n0 = bn * 128;

  u32x4 br[2][4];
  const int brow = tid >> 4, bc = tid & 15;
  auto loadB = [&](int kt, int slot) {
#pragma unroll
    for (int i = 0; i < 2; ++i) {
      const size_t off = (size_t)(kt * 64 + brow + 32 * i) * FF + n0 + bc * 8;
      br[slot][i] = *reinterpret_cast<const u32x4*>(w1 + off);
      br[slot][2 + i] = *reinterpret_cast<const u32x4*>(w3 + off);
    }
  };
  auto storeB = [&](int slot) {
#pragma unroll
    for (int i = 0; i < 2; ++i) {
      *reinterpret_cast<u32x4*>(sB + (brow + 32 * i) * UP_SB + bc * 16) = br[slot][i];
      *reinterpret_cast<u32x4*>(sB + 64 * UP_SB + (brow + 32 * i) * UP_SB + bc * 16) = br[slot][2 + i];
    }
  };
  loadB(0, 0);
  loadB(1, 1);
  LnRegs lr;
  ln_load(lr, x, m0, tid);
  ln_store<UP_SA>(lr, sA, st, tid);
  storeB(0);
  __syncthreads();

  const int wm = wv & 1, wn = wv >> 1;
  const int l31 = lane & 31, h = lane >> 5, i16 = lane & 15, q4 = i16 >> 2, p4 = i16 & 3,
            blk = (lane >> 4) & 1;
  const char* aBase = sA + (wm * 64 + l31) * UP_SA + 16 * h;
  const char* bBase = sB + (8 * h + q4) * UP_SB + (wn * 32 + 16 * blk + 4 * p4) * 2;

  f32x16 acc[2][2], cs[2];
#pragma unroll
  for (int a = 0; a < 2; ++a)
#pragma unroll
    for (int i = 0; i < 16; ++i) { acc[a][0][i] = 0.f; acc[a][1][i] = 0.f; cs[a][i] = 0.f; }
  const bf16x8 ones = {0x3f80, 0x3f80, 0x3f80, 0x3f80, 0x3f80, 0x3f80, 0x3f80, 0x3f80};

#pragma unroll
  for (int kt = 0; kt < 7; ++kt) {
#pragma unroll
    for (int ks = 0; ks < 4; ++ks) {
      bf16x8 xf[2];
#pragma unroll
      for (int mt = 0; mt < 2; ++mt) xf[mt] = lds_b128(aBase + mt * 32 * UP_SA + (kt * 64 + ks * 16) * 2);
#pragma unroll
      for (int a = 0; a < 2; ++a) {
        const bf16x8 wf = tr_frag(bBase + a * 64 * UP_SB + ks * 16 * UP_SB, UP_SB);
#pragma unroll
        for (int mt = 0; mt < 2; ++mt) acc[a][mt] = MFMA(wf, xf[mt], acc[a][mt]);
        cs[a] = MFMA(wf, ones, cs[a]);
      }
    }
    if (kt < 6) {
      __syncthreads();
      storeB((kt + 1) & 1);
      if (kt < 5) loadB(kt + 2, kt & 1);
      __syncthreads();
    }
  }

#pragma unroll
  for (int mt = 0; mt < 2; ++mt) {
    const int m = m0 + wm * 64 + mt * 32 + l31;
    const float2 mst = st[wm * 64 + mt * 32 + l31];
#pragma unroll
    for (int g = 0; g < 4; ++g) {
      float hv[4];
#pragma unroll
      for (int j = 0; j < 4; ++j) {
        const float gt = bfr(fmaf(-mst.x, cs[0][4 * g + j], acc[0][mt][4 * g + j]) * mst.y);
        const float up = bfr(fmaf(-mst.x, cs[1][4 * g + j], acc[1][mt][4 * g + j]) * mst.y);
        const float sl = bfr(gt / (1.0f + __expf(-gt)));
        hv[j] = sl * up;
      }
      u32x2 o;
      o[0] = pack2(hv[0], hv[1]);
      o[1] = pack2(hv[2], hv[3]);
      *reinterpret_cast<u32x2*>(Hb + (size_t)m * FF + n0 + wn * 32 + 8 * g + 4 * h) = o;
    }
  }
}

extern "C" void kernel_launch(void* const* d_in, const int* in_sizes, int n_in, void* d_out, int out_size,
                              void* d_ws, size_t ws_size, hipStream_t stream) {
  const u16* x = (const u16*)d_in[0];
  const u16* wqkv = (const u16*)d_in[1];
  const u16* wo = (const u16*)d_in[2];
  const u16* w1 = (const u16*)d_in[4];
  const u16* w3 = (const u16*)d_in[5];
  const u16* w2 = (const u16*)d_in[6];
  u16* out = (u16*)d_out;

  char* ws = (char*)d_ws;
  u16* QKV = (u16*)(ws);
  u16* Ob = (u16*)(ws + (6u << 20));
  u16* x1 = (u16*)(ws + (8u << 20));
  u16* x2 = (u16*)(ws + (10u << 20));
  u16* x3 = (u16*)(ws + (12u << 20));
  u16* Hb = (u16*)(ws + (16u << 20));

  const int ldsQKV = 128 * QKV_SA + 224 * QKV_SB + 1024;
  const int ldsAT = 2 * AT_BUF;
  const int ldsGR = 2 * GR_BUF;
  const int ldsUP = 128 * UP_SA + 2 * 64 * UP_SB + 1024;
  (void)hipFuncSetAttribute((const void*)k_ln_qkv, hipFuncAttributeMaxDynamicSharedMemorySize, ldsQKV);
  (void)hipFuncSetAttribute((const void*)k_attn, hipFuncAttributeMaxDynamicSharedMemorySize, ldsAT);
  (void)hipFuncSetAttribute((const void*)k_gemm_res<448>, hipFuncAttributeMaxDynamicSharedMemorySize, ldsGR);
  (void)hipFuncSetAttribute((const void*)k_gemm_res<1792>, hipFuncAttributeMaxDynamicSharedMemorySize, ldsGR);
  (void)hipFuncSetAttribute((const void*)k_ln_up, hipFuncAttributeMaxDynamicSharedMemorySize, ldsUP);

  const u16* xin = x;
  for (int layer = 0; layer < 2; ++layer) {
    u16* xa = layer == 0 ? x1 : x3;
    u16* xb = layer == 0 ? x2 : out;
    k_ln_qkv<<<224, 512, ldsQKV, stream>>>(xin, wqkv, QKV);
    k_attn<<<256, 512, ldsAT, stream>>>(QKV, Ob);
    k_gemm_res<448><<<224, 512, ldsGR, stream>>>(Ob, wo, xin, xa);
    k_ln_up<<<224, 512, ldsUP, stream>>>(xa, w1, w3, Hb);
    k_gemm_res<1792><<<224, 512, ldsGR, stream>>>(Hb, w2, xa, xb);
    xin = xb;
  }
}
```

```cpp
#include <hip/hip_runtime.h>
#include <hip/hip_bf16.h>
#include <stdint.h>


#define S_LEN 2048
#define DM 448
#define NH 16
#define DH 28
#define FF 1792

typedef __attribute__((ext_vector_type(8))) short bf16x8;
typedef __attribute__((ext_vector_type(4))) short s16x4;
typedef __attribute__((ext_vector_type(16))) float f32x16;
typedef __attribute__((ext_vector_type(4))) unsigned u32x4;
typedef __attribute__((ext_vector_type(2))) unsigned u32x2;
typedef unsigned short u16;

#define DI __device__ __forceinline__
#define MFMA(a, b, c) __builtin_amdgcn_mfma_f32_32x32x16_bf16((a), (b), (c), 0, 0, 0)

DI float bf2f(u16 v) { return __uint_as_float(((unsigned)v) << 16); }
DI u16 f2bf(float f) {
  __hip_bfloat16 b = __float2bfloat16(f);
  return *reinterpret_cast<u16*>(&b);
}
DI float bfr(float f) { return bf2f(f2bf(f)); }
typedef __attribute__((ext_vector_type(2))) float f32x2;
typedef __attribute__((ext_vector_type(2))) __bf16 bf16x2;
DI unsigned pack2(float lo, float hi) {
  const f32x2 v = {lo, hi};
  return __builtin_bit_cast(unsigned, __builtin_convertvector(v, bf16x2));
}

DI s16x4 tr_read(const char* p) {
  return __builtin_amdgcn_ds_read_tr16_b64_v4i16(
      (__attribute__((address_space(3))) s16x4*)(p));
}
DI bf16x8 tr_frag(const char* p, int stride) {
  s16x4 lo = tr_read(p);
  s16x4 hi = tr_read(p + 4 * stride);
  return __builtin_shufflevector(lo, hi, 0, 1, 2, 3, 4, 5, 6, 7);
}
DI bf16x8 lds_b128(const char* p) { return *reinterpret_cast<const bf16x8*>(p); }

struct LnRegs { u32x4 v[2][7]; };
DI void ln_load(LnRegs& r, const u16* __restrict__ x, int m0, int tid) {
  const int sub = tid & 7;
#pragma unroll
  for (int pass = 0; pass < 2; ++pass) {
    const u16* xr = x + (size_t)(m0 + pass * 64 + (tid >> 3)) * DM;
#pragma unroll
    for (int c = 0; c < 7; ++c) r.v[pass][c] = *reinterpret_cast<const u32x4*>(xr + (c * 8 + sub) * 8);
  }
}
template <int SA>
DI void ln_store(const LnRegs& r, char* sA, float* sinv, int tid) {
  const int sub = tid & 7;
#pragma unroll
  for (int pass = 0; pass < 2; ++pass) {
    const int row = pass * 64 + (tid >> 3);
    float sum = 0.f;
#pragma unroll
    for (int c = 0; c < 7; ++c)
#pragma unroll
      for (int j = 0; j < 4; ++j)
        sum += __uint_as_float(r.v[pass][c][j] << 16) + __uint_as_float(r.v[pass][c][j] & 0xffff0000u);
    sum += __shfl_xor(sum, 1);
    sum += __shfl_xor(sum, 2);
    sum += __shfl_xor(sum, 4);
    const float mean = sum * (1.0f / DM);
    const float mean_b = bfr(mean);
    float sq = 0.f;
#pragma unroll
    for (int c = 0; c < 7; ++c) {
      u32x4 o;
#pragma unroll
      for (int j = 0; j < 4; ++j) {
        const float f0 = __uint_as_float(r.v[pass][c][j] << 16);
        const float f1 = __uint_as_float(r.v[pass][c][j] & 0xffff0000u);
        const float d0 = f0 - mean, d1 = f1 - mean;
        sq = fmaf(d0, d0, fmaf(d1, d1, sq));
        o[j] = pack2(f0 - mean_b, f1 - mean_b);
      }
      *reinterpret_cast<u32x4*>(sA + row * SA + (c * 8 + sub) * 16) = o;
    }
    sq += __shfl_xor(sq, 1);
    sq += __shfl_xor(sq, 2);
    sq += __shfl_xor(sq, 4);
    if (sub == 0) sinv[row] = 1.0f / bfr(sqrtf(bfr(bfr(sq * (1.0f / DM)) + 1e-5f)));
  }
}

#define QKV_SA 912
#define QKV_SB 192
__global__ __launch_bounds__(512) void k_ln_qkv(const u16* __restrict__ x, const u16* __restrict__ w,
                                                u16* __restrict__ QKV) {
  extern __shared__ __attribute__((aligned(16))) char smem[];
  char* sA = smem;
  char* sB = smem + 128 * QKV_SA;
  float* sinv = reinterpret_cast<float*>(smem + 128 * QKV_SA + 224 * QKV_SB);
  const int tid = threadIdx.x, lane = tid & 63, wv = tid >> 6;
  const int bm = blockIdx.x & 15, bn = blockIdx.x >> 4;
  const int m0 = bm * 128, n0 = bn * 96;
  const int wr = wv & 3, kp = wv >> 2;

  u32x4 br[11];
#pragma unroll
  for (int i = 0; i < 11; ++i) {
    const int c = tid + 512 * i, r = c / 12, c16 = c % 12;
    if (i < 10 || tid < 256)
      br[i] = *reinterpret_cast<const u32x4*>(w + (size_t)r * (3 * DM) + n0 + c16 * 8);
  }
  LnRegs lr;
  ln_load(lr, x, m0, tid);
  ln_store<QKV_SA>(lr, sA, sinv, tid);
#pragma unroll
  for (int i = 0; i < 6; ++i) {
    const int c = tid + 512 * i, r = c / 12, c16 = c % 12;
    if (c < 2688) *reinterpret_cast<u32x4*>(sB + r * QKV_SB + c16 * 16) = br[i];
  }
  __syncthreads();

  f32x16 acc[3];
#pragma unroll
  for (int t = 0; t < 3; ++t)
#pragma unroll
    for (int i = 0; i < 16; ++i) acc[t][i] = 0.f;

  const int l31 = lane & 31, h = lane >> 5, i16 = lane & 15, q4 = i16 >> 2, p4 = i16 & 3,
            blk = (lane >> 4) & 1;
  const char* aBase = sA + (wr * 32 + l31) * QKV_SA + 16 * h + kp * 32;
  const char* bBase = sB + (kp * 16 + 8 * h + q4) * QKV_SB + (16 * blk + 4 * p4) * 2;

#pragma unroll
  for (int half = 0; half < 2; ++half) {
    if (half == 1) {
      __syncthreads();
#pragma unroll
      for (int i = 5; i < 11; ++i) {
        const int c = tid + 512 * i, r = c / 12, c16 = c % 12;
        if (c >= 2688 && c < 5376) *reinterpret_cast<u32x4*>(sB + (r - 224) * QKV_SB + c16 * 16) = br[i];
      }
      __syncthreads();
    }
#pragma unroll
    for (int ks = 0; ks < 7; ++ks) {
      const bf16x8 xf = lds_b128(aBase + (half * 224 + ks * 32) * 2);
#pragma unroll
      for (int t = 0; t < 3; ++t) {
        const bf16x8 wf = tr_frag(bBase + ks * 32 * QKV_SB + t * 64, QKV_SB);
        acc[t] = MFMA(wf, xf, acc[t]);
      }
    }
  }

  __syncthreads();
  float* red = reinterpret_cast<float*>(sA);
#pragma unroll
  for (int t = 0; t < 3; ++t)
#pragma unroll
    for (int i = 0; i < 16; ++i) {
      const int r = t * 16 + i;
      const int owner = r < 24 ? 0 : 1;
      if (owner != kp) red[((wr * 2 + kp) * 24 + (r % 24)) * 64 + lane] = acc[t][i];
    }
  __syncthreads();
  {
    const int m = m0 + wr * 32 + l31;
    const float inv = sinv[wr * 32 + l31];
#pragma unroll
    for (int t = 0; t < 3; ++t) {
      const int ns = n0 + 32 * t;
      const int which = ns / DM, cb = ns % DM;
      const float sc = which == 0 ? inv * (0.18898223650461362f * 1.4426950408889634f) : inv;
#pragma unroll
      for (int g = 0; g < 4; ++g) {
        const int r0 = t * 16 + 4 * g;
        const int owner = r0 < 24 ? 0 : 1;
        if (owner == kp) {
          float v[4];
#pragma unroll
          for (int j = 0; j < 4; ++j)
            v[j] = (acc[t][4 * g + j] + red[((wr * 2 + (1 - kp)) * 24 + ((r0 + j) % 24)) * 64 + lane]) * sc;
          const int c = cb + 8 * g + 4 * h;
          const int head = c / DH, d = c % DH;
          u32x2 o;
          o[0] = pack2(v[0], v[1]);
          o[1] = pack2(v[2], v[3]);
          u16* p = QKV + ((size_t)(which * NH + head) * S_LEN + m) * 32 + d;
          *reinterpret_cast<u32x2*>(p) = o;
          if (d == 24) *reinterpret_cast<u32x2*>(p + 4) = (u32x2){which == 2 ? 0x3f80u : 0u, 0u};
        }
      }
    }
  }
}

#define AT_SK 80
#define AT_SV 64
#define AT_KB (128 * AT_SK)
#define AT_VB (128 * AT_SV)
#define AT_BUF (2 * AT_KB + 2 * AT_VB)
__global__ __launch_bounds__(512) void k_attn(const u16* __restrict__ QKV, u16* __restrict__ O) {
  extern __shared__ __attribute__((aligned(16))) char smem[];
  const int tid = threadIdx.x, lane = tid & 63, wv = tid >> 6;
  const int bid = blockIdx.x;
  const int head = (bid & 7) * 2 + ((bid >> 3) & 1), qt = bid >> 4;
  const int l31 = lane & 31, h = lane >> 5, i16 = lane & 15, q4 = i16 >> 2, p4 = i16 & 3,
            blk = (lane >> 4) & 1;
  const int qs = wv & 3, kh = wv >> 2;

  const u16* Qh = QKV + (size_t)head * S_LEN * 32;
  const u16* Kh = QKV + (size_t)(NH + head) * S_LEN * 32;
  const u16* Vh = QKV + (size_t)(2 * NH + head) * S_LEN * 32;

  const int qrow = qt * 128 + qs * 32 + l31;
  const u16* qp = Qh + (size_t)qrow * 32 + 8 * h;
  const bf16x8 qf0 = *reinterpret_cast<const bf16x8*>(qp);
  const bf16x8 qf1 = *reinterpret_cast<const bf16x8*>(qp + 16);

  u32x4 kr[2], vr[2];
  auto loadKV = [&](int it) {
#pragma unroll
    for (int i = 0; i < 2; ++i) {
      const size_t off = (size_t)(i * 1024 + it * 128) * 32 + tid * 8;
      kr[i] = *reinterpret_cast<const u32x4*>(Kh + off);
      vr[i] = *reinterpret_cast<const u32x4*>(Vh + off);
    }
  };
  auto storeKV = [&](int buf) {
    char* b = smem + buf * AT_BUF;
#pragma unroll
    for (int i = 0; i < 2; ++i) {
      *reinterpret_cast<u32x4*>(b + i * AT_KB + (tid >> 2) * AT_SK + (tid & 3) * 16) = kr[i];
      *reinterpret_cast<u32x4*>(b + 2 * AT_KB + i * AT_VB + tid * 16) = vr[i];
    }
  };
  loadKV(0);
  storeKV(0);
  __syncthreads();

  float m = 0.f;
  f32x16 negm, o;
#pragma unroll
  for (int i = 0; i < 16; ++i) { negm[i] = 0.f; o[i] = 0.f; }

#pragma unroll 1
  for (int it = 0; it < 8; ++it) {
    if (it < 7) loadKV(it + 1);
    const char* kb = smem + (it & 1) * AT_BUF + kh * AT_KB + l31 * AT_SK + 16 * h;
    const char* vb = smem + (it & 1) * AT_BUF + 2 * AT_KB + kh * AT_VB + (4 * h + q4) * AT_SV +
                     (16 * blk + 4 * p4) * 2;
    f32x16 s[4];
#pragma unroll
    for (int t = 0; t < 4; ++t) {
      s[t] = MFMA(lds_b128(kb + t * 32 * AT_SK), qf0, negm);
      s[t] = MFMA(lds_b128(kb + t * 32 * AT_SK + 32), qf1, s[t]);
    }
    float mx = s[0][0];
#pragma unroll
    for (int t = 0; t < 4; ++t)
#pragma unroll
      for (int i = 0; i < 16; ++i) mx = fmaxf(mx, s[t][i]);
    mx = fmaxf(mx, __shfl_xor(mx, 32));
    const float delta = it == 0 ? mx : fmaxf(mx, 0.f);
    if (__any(delta != 0.f)) {
      const float alpha = __builtin_amdgcn_exp2f(-delta);
      m += delta;
#pragma unroll
      for (int i = 0; i < 16; ++i) { negm[i] = -m; o[i] *= alpha; }
#pragma unroll
      for (int t = 0; t < 4; ++t)
#pragma unroll
        for (int i = 0; i < 16; ++i) s[t][i] -= delta;
    }
#pragma unroll
    for (int t = 0; t < 4; ++t) {
#pragma unroll
      for (int sp = 0; sp < 2; ++sp) {
        u32x4 pk;
#pragma unroll
        for (int j = 0; j < 4; ++j)
          pk[j] = pack2(__builtin_amdgcn_exp2f(s[t][8 * sp + 2 * j]), __builtin_amdgcn_exp2f(s[t][8 * sp + 2 * j + 1]));
        const bf16x8 pf = __builtin_bit_cast(bf16x8, pk);
        const s16x4 lo = tr_read(vb + (t * 32 + sp * 16) * AT_SV);
        const s16x4 hi = tr_read(vb + (t * 32 + sp * 16 + 8) * AT_SV);
        const bf16x8 vf = __builtin_shufflevector(lo, hi, 0, 1, 2, 3, 4, 5, 6, 7);
        o = MFMA(vf, pf, o);
      }
    }
    if (it < 7) storeKV((it + 1) & 1);
    __syncthreads();
  }

  float* red = reinterpret_cast<float*>(smem);
  if (kh == 1) {
    red[(qs * 17 + 16) * 64 + lane] = m;
#pragma unroll
    for (int i = 0; i < 16; ++i) red[(qs * 17 + i) * 64 + lane] = o[i];
  }
  __syncthreads();
  if (kh == 0) {
    const float m2 = red[(qs * 17 + 16) * 64 + lane];
    const float mn = fmaxf(m, m2);
    const float a1 = __builtin_amdgcn_exp2f(m - mn);
    const float a2 = __builtin_amdgcn_exp2f(m2 - mn);
#pragma unroll
    for (int i = 0; i < 16; ++i) o[i] = o[i] * a1 + red[(qs * 17 + i) * 64 + lane] * a2;
    const float l = __shfl(o[12], l31 + 32);
    const float inv = 1.0f / l;
    u16* op = O + (size_t)qrow * DM + head * DH;
#pragma unroll
    for (int g = 0; g < 4; ++g) {
      const int d = 8 * g + 4 * h;
      if (d < DH) {
        u32x2 v;
        v[0] = pack2(o[4 * g] * inv, o[4 * g + 1] * inv);
        v[1] = pack2(o[4 * g + 2] * inv, o[4 * g + 3] * inv);
        *reinterpret_cast<u32x2*>(op + d) = v;
      }
    }
  }
}

#define GR_SA 464
#define GR_SB 192
#define GR_BUF (64 * GR_SA + 224 * GR_SB)
template <int KTOT>
__global__ __launch_bounds__(512) void k_gemm_res(const u16* __restrict__ A, const u16* __restrict__ W,
                                                  const u16* __restrict__ res, u16* __restrict__ out) {
  extern __shared__ __attribute__((aligned(16))) char smem[];
  const int tid = threadIdx.x, lane = tid & 63, wv = tid >> 6;
  const int bm = 2 * ((blockIdx.x & 7) + 8 * ((blockIdx.x >> 4) & 1)) + ((blockIdx.x >> 3) & 1);
  const int bn = blockIdx.x >> 5;
  const int m0 = bm * 64, n0 = bn * 64;
  constexpr int NC = KTOT / 224;
  constexpr int PD = NC < 3 ? NC : 3;

  const int wm = wv & 1, wn = (wv >> 1) & 1, kp = wv >> 2;
  const int l31 = lane & 31, h = lane >> 5, i16 = lane & 15, q4 = i16 >> 2, p4 = i16 & 3,
            blk = (lane >> 4) & 1;

  u32x4 ar[PD][4], br[PD][4];
  auto load = [&](int kc, int slot) {
#pragma unroll
    for (int i = 0; i < 4; ++i) {
      const int c = tid + 512 * i;
      if (i < 3 || tid < 256) {
        const int r = c / 28, c16 = c % 28;
        ar[slot][i] = *reinterpret_cast<const u32x4*>(A + (size_t)(m0 + r) * KTOT + kc * 224 + c16 * 8);
        const int rb = c >> 3, cb = c & 7;
        br[slot][i] = *reinterpret_cast<const u32x4*>(W + (size_t)(kc * 224 + rb) * DM + n0 + cb * 8);
      }
    }
  };
  auto store = [&](int slot, int buf) {
    char* b = smem + buf * GR_BUF;
#pragma unroll
    for (int i = 0; i < 4; ++i) {
      const int c = tid + 512 * i;
      if (i < 3 || tid < 256) {
        const int r = c / 28, c16 = c % 28;
        *reinterpret_cast<u32x4*>(b + r * GR_SA + c16 * 16) = ar[slot][i];
        const int rb = c >> 3, cb = c & 7;
        *reinterpret_cast<u32x4*>(b + 64 * GR_SA + rb * GR_SB + cb * 16) = br[slot][i];
      }
    }
  };
#pragma unroll
  for (int i = 0; i < PD; ++i) load(i, i);
  const int m = m0 + wm * 32 + l31;
  u32x2 rr[4];
#pragma unroll
  for (int g = 0; g < 4; ++g)
    rr[g] = *reinterpret_cast<const u32x2*>(res + (size_t)m * DM + n0 + wn * 32 + 8 * g + 4 * h);
  store(0, 0);
  if (PD < NC) load(PD, 0);
  __syncthreads();

  const int aOff = (wm * 32 + l31) * GR_SA + 16 * h + kp * 32;
  const int bOff = 64 * GR_SA + (kp * 16 + 8 * h + q4) * GR_SB + (wn * 32 + 16 * blk + 4 * p4) * 2;

  f32x16 acc, acc2;
#pragma unroll
  for (int i = 0; i < 16; ++i) { acc[i] = 0.f; acc2[i] = 0.f; }

#pragma unroll
  for (int kc = 0; kc < NC; ++kc) {
    const char* b = smem + (kc & 1) * GR_BUF;
#pragma unroll
    for (int ks = 0; ks < 7; ++ks) {
      const bf16x8 xf = lds_b128(b + aOff + ks * 64);
      const bf16x8 wf = tr_frag(b + bOff + ks * 32 * GR_SB, GR_SB);
      if (ks & 1) acc2 = MFMA(wf, xf, acc2); else acc = MFMA(wf, xf, acc);
    }
    if (kc + 1 < NC) {
      store((kc + 1) % PD, (kc + 1) & 1);
      if (kc + 1 + PD < NC) load(kc + 1 + PD, (kc + 1) % PD);
      __syncthreads();
    }
  }
#pragma unroll
  for (int i = 0; i < 16; ++i) acc[i] += acc2[i];

  __syncthreads();
  float* red = reinterpret_cast<float*>(smem);
  if (kp == 1) {
#pragma unroll
    for (int i = 0; i < 16; ++i) red[((wv & 3) * 16 + i) * 64 + lane] = acc[i];
  }
  __syncthreads();
  if (kp == 0) {
#pragma unroll
    for (int i = 0; i < 16; ++i) acc[i] += red[((wv & 3) * 16 + i) * 64 + lane];
#pragma unroll
    for (int g = 0; g < 4; ++g) {
      const size_t off = (size_t)m * DM + n0 + wn * 32 + 8 * g + 4 * h;
      u32x2 o;
      o[0] = pack2(__uint_as_float(rr[g][0] << 16) + bfr(acc[4 * g]),
                   __uint_as_float(rr[g][0] & 0xffff0000u) + bfr(acc[4 * g + 1]));
      o[1] = pack2(__uint_as_float(rr[g][1] << 16) + bfr(acc[4 * g + 2]),
                   __uint_as_float(rr[g][1] & 0xffff0000u) + bfr(acc[4 * g + 3]));
      *reinterpret_cast<u32x2*>(out + off) = o;
    }
  }
}

#define UP_SA 912
#define UP_SB 320
__global__ __launch_bounds__(512) void k_ln_up(const u16* __restrict__ x, const u16* __restrict__ w1,
                                               const u16* __restrict__ w3, u16* __restrict__ Hb) {
  extern __shared__ __attribute__((aligned(16))) char smem[];
  char* sA = smem;
  char* sB = smem + 128 * UP_SA;
  float* sinv = reinterpret_cast<float*>(smem + 128 * UP_SA + 2 * 64 * UP_SB);
  const int tid = threadIdx.x, lane = tid & 63, wv = tid >> 6;
  const int bm = blockIdx.x & 15, bn = blockIdx.x >> 4;
  const int m0 = bm * 128, n0 = bn * 128;

  u32x4 br[2][4];
  const int brow = tid >> 4, bc = tid & 15;
  auto loadB = [&](int kt, int slot) {
#pragma unroll
    for (int i = 0; i < 2; ++i) {
      const size_t off = (size_t)(kt * 64 + brow + 32 * i) * FF + n0 + bc * 8;
      br[slot][i] = *reinterpret_cast<const u32x4*>(w1 + off);
      br[slot][2 + i] = *reinterpret_cast<const u32x4*>(w3 + off);
    }
  };
  auto storeB = [&](int slot) {
#pragma unroll
    for (int i = 0; i < 2; ++i) {
      *reinterpret_cast<u32x4*>(sB + (brow + 32 * i) * UP_SB + bc * 16) = br[slot][i];
      *reinterpret_cast<u32x4*>(sB + 64 * UP_SB + (brow + 32 * i) * UP_SB + bc * 16) = br[slot][2 + i];
    }
  };
  loadB(0, 0);
  loadB(1, 1);
  LnRegs lr;
  ln_load(lr, x, m0, tid);
  ln_store<UP_SA>(lr, sA, sinv, tid);
  storeB(0);
  __syncthreads();

  const int wm = wv & 1, wn = wv >> 1;
  const int l31 = lane & 31, h = lane >> 5, i16 = lane & 15, q4 = i16 >> 2, p4 = i16 & 3,
            blk = (lane >> 4) & 1;
  const char* aBase = sA + (wm * 64 + l31) * UP_SA + 16 * h;
  const char* bBase = sB + (8 * h + q4) * UP_SB + (wn * 32 + 16 * blk + 4 * p4) * 2;

  f32x16 acc[2][2];
#pragma unroll
  for (int a = 0; a < 2; ++a)
#pragma unroll
    for (int c = 0; c < 2; ++c)
#pragma unroll
      for (int i = 0; i < 16; ++i) acc[a][c][i] = 0.f;

#pragma unroll
  for (int kt = 0; kt < 7; ++kt) {
#pragma unroll
    for (int ks = 0; ks < 4; ++ks) {
      bf16x8 xf[2];
#pragma unroll
      for (int mt = 0; mt < 2; ++mt) xf[mt] = lds_b128(aBase + mt * 32 * UP_SA + (kt * 64 + ks * 16) * 2);
#pragma unroll
      for (int a = 0; a < 2; ++a) {
        const bf16x8 wf = tr_frag(bBase + a * 64 * UP_SB + ks * 16 * UP_SB, UP_SB);
#pragma unroll
        for (int mt = 0; mt < 2; ++mt) acc[a][mt] = MFMA(wf, xf[mt], acc[a][mt]);
      }
    }
    if (kt < 6) {
      __syncthreads();
      storeB((kt + 1) & 1);
      if (kt < 5) loadB(kt + 2, kt & 1);
      __syncthreads();
    }
  }

#pragma unroll
  for (int mt = 0; mt < 2; ++mt) {
    const int m = m0 + wm * 64 + mt * 32 + l31;
    const float inv = sinv[wm * 64 + mt * 32 + l31];
#pragma unroll
    for (int g = 0; g < 4; ++g) {
      float hv[4];
#pragma unroll
      for (int j = 0; j < 4; ++j) {
        const float gt = bfr(acc[0][mt][4 * g + j] * inv);
        const float up = bfr(acc[1][mt][4 * g + j] * inv);
        const float sl = bfr(gt * __builtin_amdgcn_rcpf(1.0f + __expf(-gt)));
        hv[j] = sl * up;
      }
      u32x2 o;
      o[0] = pack2(hv[0], hv[1]);
      o[1] = pack2(hv[2], hv[3]);
      *reinterpret_cast<u32x2*>(Hb + (size_t)m * FF + n0 + wn * 32 + 8 * g + 4 * h) = o;
    }
  }
}

extern "C" void kernel_launch(void* const* d_in, const int* in_sizes, int n_in, void* d_out, int out_size,
                              void* d_ws, size_t ws_size, hipStream_t stream) {
  const u16* x = (const u16*)d_in[0];
  const u16* wqkv = (const u16*)d_in[1];
  const u16* wo = (const u16*)d_in[2];
  const u16* w1 = (const u16*)d_in[4];
  const u16* w3 = (const u16*)d_in[5];
  const u16* w2 = (const u16*)d_in[6];
  u16* out = (u16*)d_out;

  char* ws = (char*)d_ws;
  u16* QKV = (u16*)(ws);
  u16* Ob = (u16*)(ws + (6u << 20));
  u16* x1 = (u16*)(ws + (8u << 20));
  u16* x2 = (u16*)(ws + (10u << 20));
  u16* x3 = (u16*)(ws + (12u << 20));
  u16* Hb = (u16*)(ws + (16u << 20));

  const int ldsQKV = 128 * QKV_SA + 224 * QKV_SB + 512;
  const int ldsAT = 2 * AT_BUF;
  const int ldsGR = 2 * GR_BUF;
  const int ldsUP = 128 * UP_SA + 2 * 64 * UP_SB + 512;
  (void)hipFuncSetAttribute((const void*)k_ln_qkv, hipFuncAttributeMaxDynamicSharedMemorySize, ldsQKV);
  (void)hipFuncSetAttribute((const void*)k_attn, hipFuncAttributeMaxDynamicSharedMemorySize, ldsAT);
  (void)hipFuncSetAttribute((const void*)k_gemm_res<448>, hipFuncAttributeMaxDynamicSharedMemorySize, ldsGR);
  (void)hipFuncSetAttribute((const void*)k_gemm_res<1792>, hipFuncAttributeMaxDynamicSharedMemorySize, ldsGR);
  (void)hipFuncSetAttribute((const void*)k_ln_up, hipFuncAttributeMaxDynamicSharedMemorySize, ldsUP);

  const u16* xin = x;
  for (int layer = 0; layer < 2; ++layer) {
    u16* xa = layer == 0 ? x1 : x3;
    u16* xb = layer == 0 ? x2 : out;
    k_ln_qkv<<<224, 512, ldsQKV, stream>>>(xin, wqkv, QKV);
    k_attn<<<256, 512, ldsAT, stream>>>(QKV, Ob);
    k_gemm_res<448><<<224, 512, ldsGR, stream>>>(Ob, wo, xin, xa);
    k_ln_up<<<224, 512, ldsUP, stream>>>(xa, w1, w3, Hb);
    k_gemm_res<1792><<<224, 512, ldsGR, stream>>>(Hb, w2, xa, xb);
    xin = xb;
  }
}
```
